```python
import torch

def _softmax(x, axis=-1):
    return torch.softmax(x, dim=axis)
def _gelu(x):
    return torch.nn.functional.gelu(x, approximate="tanh")
def _silu(x):
    return torch.nn.functional.silu(x)
def _sigmoid(x):
    return torch.sigmoid(x)
def _layernorm(x, axis=-1, eps=1e-5):
    m = x.mean(dim=axis, keepdim=True)
    v = x.var(dim=axis, keepdim=True, unbiased=False)
    return (x - m) / torch.sqrt(v + eps)
def _scatter_add(tgt, idx, src, axis=0):
    return tgt.clone().index_add_(axis, idx, src)
def _segment_sum(src, seg, n):
    out = torch.zeros((n,) + src.shape[1:], dtype=src.dtype)
    return out.index_add_(0, seg, src)
def _pad(x, pw):
    flat = tuple(v for pair in reversed(pw) for v in pair)
    return torch.nn.functional.pad(x, flat)

kernel_name = 'recurrent_scan__ssm_conv_h3__mixer_s_mlp__prompt'

def setup_inputs():

    _w = (lambda a: (lambda t: t.to(torch.bfloat16) if t.is_floating_point() else t)(torch.as_tensor(a)))
    def _A_setup():
        B, S, D = 1, 2048, 576
        ks = 4
        return {
            "q": _w((lambda *s: torch.randn(*s, dtype=torch.bfloat16) * 0.05)(B, S, D)),
            "k": _w((lambda *s: torch.randn(*s, dtype=torch.bfloat16) * 0.2)(B, S, D)),
            "v": _w((lambda *s: torch.randn(*s, dtype=torch.bfloat16) * 0.2)(B, S, D)),
            "shift_k": _w((lambda *s: torch.randn(*s, dtype=torch.bfloat16) * 0.2)(D, ks) * 0.5),
            "ssm_k": _w((lambda *s: torch.randn(*s, dtype=torch.bfloat16) * 0.2)(D, S) * (1.0 / S ** 0.5)),
            "ssm_D": _w((lambda *s: torch.randn(*s, dtype=torch.bfloat16) * 0.2)(D)),
        }
    _A_in = _A_setup()

    def _B_setup():
        B, S, D, F = 1, 2048, 576, 4608
        return {
            "x":  _w((lambda *s: torch.randn(*s, dtype=torch.bfloat16) * 0.05)(B, S, D)),
            "w1": _w((lambda *s: torch.randn(*s, dtype=torch.bfloat16) * 3.2)(D, D) * (D ** -0.5)),
            "w2": _w((lambda *s: torch.randn(*s, dtype=torch.bfloat16) * 3.2)(D, D) * (D ** -0.5)),
            "w3": _w((lambda *s: torch.randn(*s, dtype=torch.bfloat16) * 3.2)(D, F) * (D ** -0.5)),
            "w4": _w((lambda *s: torch.randn(*s, dtype=torch.bfloat16) * 3.2)(F, D) * (F ** -0.5)),
        }
    _B_in = _B_setup()

    return {
        "A_q": _A_in["q"],
        "A_k": _A_in["k"],
        "A_v": _A_in["v"],
        "A_shift_k": _A_in["shift_k"],
        "A_ssm_k": _A_in["ssm_k"],
        "A_ssm_D": _A_in["ssm_D"],
        "B_x": _B_in["x"],
        "B_w1": _B_in["w1"],
        "B_w2": _B_in["w2"],
        "B_w3": _B_in["w3"],
        "B_w4": _B_in["w4"],
    }

def reference(A_q, A_k, A_v, A_shift_k, A_ssm_k, A_ssm_D, B_x, B_w1, B_w2, B_w3, B_w4):
    def _A_ref(q, k, v, shift_k, ssm_k, ssm_D):
        B, S, D = q.shape
        ks = shift_k.shape[-1]
        k_pad = _pad(k, ((0, 0), (ks - 1, 0), (0, 0)))
        k_shift = torch.zeros_like(k)
        for t in range(ks):
            k_shift = k_shift + k_pad[:, t:t + S, :] * shift_k[:, ks - 1 - t][None, None, :]
        kv = k_shift * v
        L = 2 * S
        kv_pad = _pad(kv, ((0, 0), (0, S), (0, 0)))
        kf_pad = _pad(ssm_k, ((0, 0), (0, S)))
        kv_f = (lambda x, axes=(-2,-1): torch.fft.rfft2(x.float(), dim=axes))(kv_pad[:, None, :, :], axes=(1, 2))
        kf_f = (lambda x, axes=(-2,-1): torch.fft.rfft2(x.float(), dim=axes))(kf_pad[:, None, :], axes=(1, 2))
        kf_f = (lambda x, *d: x.permute(*d))(kf_f, 1, 2, 0)
        y_f = kv_f * kf_f[None, :, :, :]
        y = (lambda x, s, axes=(-2,-1): torch.fft.irfft2(x, s=s, dim=axes).to(torch.bfloat16))(y_f, (1, L), axes=(1, 2))[:, 0, :S, :]
        y = y + kv * ssm_D[None, None, :]
        return q * y

    def _B_ref(x, w1, w2, w3, w4):
        B, S, D = x.shape
        h = _gelu(_layernorm(x, axis=-1) @ w1)
        c = D // 4
        g0 = _pad(h[:, :, 0*c:1*c], ((0, 0), (1, 0), (0, 0)))[:, :S, :]
        g1 = _pad(h[:, :, 1*c:2*c], ((0, 0), (0, 1), (0, 0)))[:, 1:, :]
        g2 = _pad(h[:, :, 2*c:3*c], ((0, 0), (2, 0), (0, 0)))[:, :S, :]
        g3 = _pad(h[:, :, 3*c:   ], ((0, 0), (0, 2), (0, 0)))[:, 2:, :]
        hs = torch.concatenate([g0, g1, g2, g3], axis=-1) @ w2
        x1 = x + hs
        h2 = _gelu(_layernorm(x1, axis=-1) @ w3) @ w4
        return x1 + h2

    x = A_q
    xn = _layernorm(x, axis=-1)
    s = _A_ref(xn, A_k, A_v, A_shift_k, A_ssm_k, A_ssm_D)
    g = _silu(_B_ref(xn, B_w1, B_w2, B_w3, B_w4))
    return x + s * g
```

```cpp
#include <hip/hip_runtime.h>
#include <hip/hip_bf16.h>
#include <stdint.h>

typedef __attribute__((ext_vector_type(8))) __bf16 bf16x8;
typedef __attribute__((ext_vector_type(2))) __bf16 bf16x2;
typedef __attribute__((ext_vector_type(4))) float f32x4;
typedef __attribute__((ext_vector_type(2))) float f32x2;
typedef __attribute__((ext_vector_type(4))) unsigned u32x4;
typedef __attribute__((ext_vector_type(2))) unsigned u32x2;
typedef __amdgpu_buffer_rsrc_t rsrc_t;
typedef __attribute__((address_space(3))) void* lds_ptr;

#define DEVI __device__ __forceinline__

constexpr int S_ = 2048, D_ = 576, F_ = 4608;
constexpr int ACT = S_ * D_ * 2;
constexpr int WSM = D_ * D_ * 2;
constexpr int WBG = D_ * F_ * 2;
constexpr size_t O_HF = 0;
constexpr size_t O_KV = O_HF + ACT;
constexpr size_t O_X1 = O_KV + ACT;
constexpr size_t O_Z = O_X1 + ACT;
constexpr size_t O_W2 = O_Z + ACT;
constexpr size_t O_W3 = O_W2 + WSM;
constexpr size_t O_W4 = O_W3 + WBG;
constexpr size_t O_CSP = O_W4 + WBG;
constexpr size_t O_CS = O_CSP + 18 * F_ * 4;
constexpr size_t O_ST1 = O_CS + 20480;
constexpr size_t O_ST2 = O_ST1 + S_ * 8;
constexpr size_t O_PART = O_ST2 + 18 * S_ * 8;

#define ST_AUX 0
#define ST_AUX3 0

DEVI rsrc_t mk(const void* p, int bytes) {
  return __builtin_amdgcn_make_buffer_rsrc((void*)p, (short)0, bytes, 0x00020000);
}
DEVI unsigned ld32(rsrc_t r, unsigned v, unsigned s) { return __builtin_amdgcn_raw_buffer_load_b32(r, v, s, 0); }
DEVI u32x2 ld64(rsrc_t r, unsigned v, unsigned s) { return __builtin_amdgcn_raw_buffer_load_b64(r, v, s, 0); }
DEVI u32x4 ld128(rsrc_t r, unsigned v, unsigned s) { return __builtin_amdgcn_raw_buffer_load_b128(r, v, s, 0); }
template <int AUX> DEVI void st128a(u32x4 d, rsrc_t r, unsigned v, unsigned s) {
  __builtin_amdgcn_raw_buffer_store_b128(d, r, v, s, AUX);
}
DEVI void st128(u32x4 d, rsrc_t r, unsigned v, unsigned s) { st128a<ST_AUX>(d, r, v, s); }
DEVI void st64(u32x2 d, rsrc_t r, unsigned v, unsigned s) { __builtin_amdgcn_raw_buffer_store_b64(d, r, v, s, ST_AUX); }
DEVI void glds16(rsrc_t r, char* lds, unsigned v, unsigned s) {
  __builtin_amdgcn_raw_ptr_buffer_load_lds(r, (lds_ptr)lds, 16, v, s, 0, 0);
}
DEVI void glds4(rsrc_t r, char* lds, unsigned v, unsigned s) {
  __builtin_amdgcn_raw_ptr_buffer_load_lds(r, (lds_ptr)lds, 4, v, s, 0, 0);
}
DEVI unsigned pk2(float a, float b) {
  bf16x2 v = {(__bf16)a, (__bf16)b};
  return __builtin_bit_cast(unsigned, v);
}
DEVI float blo(unsigned u) { return __builtin_bit_cast(float, u << 16); }
DEVI float bhi(unsigned u) { return __builtin_bit_cast(float, u & 0xffff0000u); }
DEVI float rbf(float a) { return (float)(__bf16)a; }
DEVI f32x4 mfma(u32x4 a, u32x4 b, f32x4 c) {
  return __builtin_amdgcn_mfma_f32_16x16x32_bf16(__builtin_bit_cast(bf16x8, a), __builtin_bit_cast(bf16x8, b), c, 0, 0, 0);
}
DEVI unsigned plo(unsigned a, unsigned b) { return __builtin_amdgcn_perm(b, a, 0x05040100u); }
DEVI unsigned phi(unsigned a, unsigned b) { return __builtin_amdgcn_perm(b, a, 0x07060302u); }
DEVI float gelu_t(float x) {
  const float c0 = -2.302208198f, c1 = -0.1029432397f;
  float e = __builtin_amdgcn_exp2f(x * __builtin_fmaf(x * x, c1, c0));
  return x * __builtin_amdgcn_rcpf(1.0f + e);
}
#define WAITVM(n) asm volatile("s_waitcnt vmcnt(" #n ")" ::: "memory")
#define BAR()                                          \
  do {                                                 \
    asm volatile("s_waitcnt lgkmcnt(0)" ::: "memory"); \
    __builtin_amdgcn_s_barrier();                      \
    asm volatile("" ::: "memory");                     \
  } while (0)
#define FENCE() __builtin_amdgcn_sched_barrier(0)

template <int V> struct IC { static constexpr int v = V; };
template <int I, int N, class Fn> DEVI void sfor(Fn&& f) {
  if constexpr (I < N) {
    f(IC<I>{});
    sfor<I + 1, N>(f);
  }
}

constexpr unsigned ONES = 0x3f803f80u;
constexpr int LDS12 = 73728;

constexpr int K1_GEMM = 192, K1_KV = 96, K1_W3 = 108, K1_W2 = 27;

__global__ __launch_bounds__(384, 3) void k1(const void* __restrict__ q, const void* __restrict__ kin,
                                             const void* __restrict__ vin, const void* __restrict__ shk,
                                             const void* __restrict__ w1, const void* __restrict__ w2,
                                             const void* __restrict__ w3, char* __restrict__ ws) {
  extern __shared__ __attribute__((aligned(16))) char sm[];
  const int tid = threadIdx.x, lane = tid & 63;
  const int wv = __builtin_amdgcn_readfirstlane(tid >> 6);
  const int r = lane & 15, g = lane >> 4;
  int b = blockIdx.x;
  if (b < K1_GEMM) {
    const int i = b >> 3, Mt = (b & 7) * 4 + i / 6, jn = i % 6;
    const int pr = wv % 3, kh = wv / 3, P = jn * 3 + pr;
    const rsrc_t rq = mk(q, ACT), rw = mk(w1, WSM);
#pragma unroll
    for (int n = 0; n < 12; ++n) {
      const int L = (n * 6 + wv) * 64 + lane, row = L / 72, c = (L - row * 72) ^ ((row >> 1) & 7);
      glds16(rq, sm + (n * 6 + wv) * 1024, row * 1152 + c * 16, Mt * 73728);
    }
    FENCE();
    unsigned wr[9][8];
    const unsigned wv0 = g * 9216 + r * 4, ws0 = kh * 9 * 36864 + P * 64;
#pragma unroll
    for (int s = 0; s < 6; ++s)
#pragma unroll
      for (int j = 0; j < 8; ++j) wr[s][j] = ld32(rw, wv0, ws0 + s * 36864 + j * 1152);
    FENCE();
    WAITVM(48);
    BAR();
#pragma unroll
    for (int s = 6; s < 9; ++s)
#pragma unroll
      for (int j = 0; j < 8; ++j) wr[s][j] = ld32(rw, wv0, ws0 + s * 36864 + j * 1152);
    FENCE();
    f32x4 acc[2][4] = {}, cs[2] = {}, su = {}, sq = {}, su3 = {}, sq3 = {};
    const u32x4 ones = {ONES, ONES, ONES, ONES};
    const int swz = (r >> 1) & 7;
#pragma unroll
    for (int s = 0; s < 9; ++s) {
      const int c = (kh * 9 + s) * 4 + g;
      u32x4 x[4];
#pragma unroll
      for (int t = 0; t < 4; ++t) x[t] = *(const u32x4*)(sm + ((((t + pr) & 3) * 16 + r) * 72 + (c ^ swz)) * 16);
      u32x4 a0, a1;
#pragma unroll
      for (int p = 0; p < 4; ++p) {
        a0[p] = plo(wr[s][2 * p], wr[s][2 * p + 1]);
        a1[p] = phi(wr[s][2 * p], wr[s][2 * p + 1]);
      }
#pragma unroll
      for (int t = 0; t < 4; ++t) {
        acc[0][t] = mfma(a0, x[t], acc[0][t]);
        acc[1][t] = mfma(a1, x[t], acc[1][t]);
      }
      cs[0] = mfma(a0, ones, cs[0]);
      cs[1] = mfma(a1, ones, cs[1]);
      su = mfma(ones, x[0], su);
      sq = mfma(x[0], x[0], sq);
      if (pr == 0) {
        su3 = mfma(ones, x[3], su3);
        sq3 = mfma(x[3], x[3], sq3);
      }
    }
    BAR();
    {
      char* e = sm + wv * 6144 + lane * 16;
#pragma unroll
      for (int n = 0; n < 2; ++n) {
        *(f32x4*)(e + (n * 2 + 0) * 1024) = kh ? acc[n][0] : acc[n][2];
        *(f32x4*)(e + (n * 2 + 1) * 1024) = kh ? acc[n][1] : acc[n][3];
        *(f32x4*)(e + (4 + n) * 1024) = cs[n];
      }
      float* st = (float*)(sm + 36864) + kh * 128;
      if (g == (r >> 2)) {
        const int q3 = r & 3;
        const float d = q3 == 0 ? sq[0] : q3 == 1 ? sq[1] : q3 == 2 ? sq[2] : sq[3];
        *(f32x2*)(st + (pr * 16 + r) * 2) = f32x2{su[0], d};
        if (pr == 0) {
          const float d3 = q3 == 0 ? sq3[0] : q3 == 1 ? sq3[1] : q3 == 2 ? sq3[2] : sq3[3];
          *(f32x2*)(st + (48 + r) * 2) = f32x2{su3[0], d3};
        }
      }
    }
    BAR();
    {
      const char* e = sm + (kh ? wv - 3 : wv + 3) * 6144 + lane * 16;
      const float* st = (const float*)(sm + 36864);
      const rsrc_t rh = mk(ws + O_HF, ACT), rs1 = mk(ws + O_ST1, S_ * 8);
      const int grp = (P * 4 + g) / 18;
      const int sh = grp == 0 ? 1 : grp == 1 ? -1 : grp == 2 ? 2 : -2;
      f32x4 c0 = cs[0] + *(const f32x4*)(e + 4096), c1 = cs[1] + *(const f32x4*)(e + 5120);
#pragma unroll
      for (int u = 0; u < 2; ++u) {
        const int mt = (2 * kh + u + pr) & 3;
        f32x4 v0 = (kh ? acc[0][2 + u] : acc[0][u]) + *(const f32x4*)(e + u * 1024);
        f32x4 v1 = (kh ? acc[1][2 + u] : acc[1][u]) + *(const f32x4*)(e + (2 + u) * 1024);
        const f32x2 s0 = *(const f32x2*)(st + (mt * 16 + r) * 2), s1 = *(const f32x2*)(st + 128 + (mt * 16 + r) * 2);
        const float mean = (s0[0] + s1[0]) * (1.0f / D_);
        const float var = fmaxf((s0[1] + s1[1]) * (1.0f / D_) - mean * mean, 0.0f);
        const float dn = rbf(__builtin_sqrtf(rbf(rbf(var) + 1e-5f)));
        const float r1 = 1.0f / dn;
        const float sc = r1 / __builtin_sqrtf(var * r1 * r1 + 1e-5f);
        const float nm = -mean * sc;
        const int row = Mt * 64 + mt * 16 + r;
        if (P == 0 && g == 0) st64(__builtin_bit_cast(u32x2, f32x2{rbf(mean), r1}), rs1, row * 8, 0);
        u32x4 o;
#pragma unroll
        for (int p = 0; p < 4; ++p)
          o[p] = pk2(gelu_t(__builtin_fmaf(v0[p], sc, nm * c0[p])), gelu_t(__builtin_fmaf(v1[p], sc, nm * c1[p])));
        const int dr = row + sh, dw = dr & (S_ - 1);
        if (dr != dw) o = u32x4{0, 0, 0, 0};
        st128(o, rh, (dw >> 4) * 18432 + (g * 16 + (dw & 15)) * 16, P * 1024);
      }
    }
    return;
  }
  b -= K1_GEMM;
  if (b < K1_KV) {
    const int un = b * 6 + wv, G = un % 9, sb = un / 9;
    const rsrc_t rk = mk(kin, ACT), rv = mk(vin, ACT), rsk = mk(shk, D_ * 8), ro = mk(ws + O_KV, ACT);
    const int row0 = sb * 32 + g * 8;
    const unsigned off = row0 * 1152 + G * 128 + r * 8;
    u32x2 kk[11], vv[8];
#pragma unroll
    for (int j = 0; j < 3; ++j) kk[j] = ld64(rk, row0 == 0 ? 0x80000000u : off - (3 - j) * 1152, 0);
#pragma unroll
    for (int j = 0; j < 8; ++j) {
      kk[3 + j] = ld64(rk, off, j * 1152);
      vv[j] = ld64(rv, off, j * 1152);
    }
    const u32x4 s0 = ld128(rsk, (G * 64 + r * 4) * 8, 0), s1 = ld128(rsk, (G * 64 + r * 4) * 8 + 16, 0);
    char* e = sm + wv * 4096;
#pragma unroll
    for (int t = 0; t < 4; ++t) {
      const unsigned sa = t < 2 ? s0[2 * t] : s1[2 * t - 4], sb2 = t < 2 ? s0[2 * t + 1] : s1[2 * t - 3];
      const float c0 = blo(sa), c1 = bhi(sa), c2 = blo(sb2), c3 = bhi(sb2);
      float kf[11], o[8];
#pragma unroll
      for (int j = 0; j < 11; ++j) kf[j] = (t & 1) ? bhi(kk[j][t >> 1]) : blo(kk[j][t >> 1]);
#pragma unroll
      for (int j = 0; j < 8; ++j) {
        const float ks = __builtin_fmaf(kf[j + 3], c0, __builtin_fmaf(kf[j + 2], c1, __builtin_fmaf(kf[j + 1], c2, kf[j] * c3)));
        o[j] = ks * ((t & 1) ? bhi(vv[j][t >> 1]) : blo(vv[j][t >> 1]));
      }
      const u32x4 w = {pk2(o[7], o[6]), pk2(o[5], o[4]), pk2(o[3], o[2]), pk2(o[1], o[0])};
      *(u32x4*)(e + (r * 16 + ((t * 4 + g) ^ r)) * 16) = w;
    }
    asm volatile("s_waitcnt lgkmcnt(0)" ::: "memory");
#pragma unroll
    for (int n = 0; n < 4; ++n) {
      const int hi = n * 4 + g;
      const u32x4 w = *(const u32x4*)(e + (hi * 16 + (r ^ hi)) * 16);
      st128(w, ro, (n * 8 + (lane >> 3)) * 8192 + (lane & 7) * 16, G * 32 * 8192 + sb * 128);
    }
    return;
  }
  b -= K1_KV;
  if (b < K1_W3) {
    const int un = b * 6 + wv, G = un % 36, ks = un / 36;
    const rsrc_t rw = mk(w3, WBG), ro = mk(ws + O_W3, WBG), rc = mk(ws + O_CSP, 18 * F_ * 4);
    u32x4 w[8];
#pragma unroll
    for (int j = 0; j < 8; ++j) w[j] = ld128(rw, g * 8 * 9216 + r * 16, (ks * 32 + j) * 9216 + G * 256);
    const u32x4 ones = {ONES, ONES, ONES, ONES};
    f32x4 mine = {};
#pragma unroll
    for (int t = 0; t < 8; ++t) {
      u32x4 a;
#pragma unroll
      for (int p = 0; p < 4; ++p)
        a[p] = (t & 1) ? phi(w[2 * p][t >> 1], w[2 * p + 1][t >> 1]) : plo(w[2 * p][t >> 1], w[2 * p + 1][t >> 1]);
      const int T = G * 8 + t;
      st128(a, ro, lane * 16, ((T / 3) * 54 + ks * 3 + T % 3) * 1024);
      const f32x4 c = mfma(a, ones, f32x4{0, 0, 0, 0});
      if (r == t) mine = c;
    }
    if (r < 8) st128(__builtin_bit_cast(u32x4, mine), rc, r * 64 + g * 16, ks * F_ * 4 + G * 512);
    return;
  }
  b -= K1_W3;
  {
    const int un = b * 6 + wv, G = un % 9, ks = un / 9;
    const rsrc_t rw = mk(w2, WSM), ro = mk(ws + O_W2, WSM);
    unsigned w[2][8];
#pragma unroll
    for (int j = 0; j < 8; ++j)
#pragma unroll
      for (int h = 0; h < 2; ++h) w[h][j] = ld32(rw, g * 9216 + r * 4, (ks * 32 + j) * 1152 + G * 128 + h * 64);
#pragma unroll
    for (int h = 0; h < 2; ++h) {
      u32x4 a0, a1;
#pragma unroll
      for (int p = 0; p < 4; ++p) {
        a0[p] = plo(w[h][2 * p], w[h][2 * p + 1]);
        a1[p] = phi(w[h][2 * p], w[h][2 * p + 1]);
      }
      st128(a0, ro, lane * 16, ((G * 2 + h) * 18 + ks) * 2048);
      st128(a1, ro, lane * 16, ((G * 2 + h) * 18 + ks) * 2048 + 1024);
    }
  }
}

constexpr int K2_GEMM = 192, K2_CONV = 288, K2_CS = 12;
constexpr int CV_KV = 0, CV_E = 4608, CV_O = CV_E + 4224, CV_CH = CV_O + 4224, CV_Y = 2 * CV_CH;

__global__ __launch_bounds__(384, 3) void k2(const void* __restrict__ q, const void* __restrict__ ssk,
                                             const void* __restrict__ ssd, const void* __restrict__ w4,
                                             char* __restrict__ ws) {
  extern __shared__ __attribute__((aligned(16))) char sm[];
  const int tid = threadIdx.x, lane = tid & 63;
  const int wv = __builtin_amdgcn_readfirstlane(tid >> 6);
  const int r = lane & 15, g = lane >> 4;
  int b = blockIdx.x;
  if (b < K2_GEMM) {
    const int i = b >> 3, Mt = (b & 7) * 4 + i / 6, jn = i % 6;
    const int pr = wv % 3, kh = wv / 3, P = jn * 3 + pr;
    const rsrc_t rh = mk(ws + O_HF, ACT), rw = mk(ws + O_W2, WSM), rq = mk(q, ACT), rs1 = mk(ws + O_ST1, S_ * 8);
#pragma unroll
    for (int n = 0; n < 12; ++n) glds16(rh, sm + (n * 6 + wv) * 1024, lane * 16, Mt * 73728 + (n * 6 + wv) * 1024);
    FENCE();
    u32x4 a[9][2];
#pragma unroll
    for (int s = 0; s < 9; ++s)
#pragma unroll
      for (int t = 0; t < 2; ++t) a[s][t] = ld128(rw, lane * 16, (P * 18 + kh * 9 + s) * 2048 + t * 1024);
    u32x4 xq[2];
    u32x2 s1[2];
#pragma unroll
    for (int u = 0; u < 2; ++u) {
      const int row0 = Mt * 64 + (2 * kh + u) * 16;
      xq[u] = ld128(rq, r * 1152 + g * 16, row0 * 1152 + P * 64);
      s1[u] = ld64(rs1, r * 8, row0 * 8);
    }
    FENCE();
    WAITVM(22);
    BAR();
    f32x4 acc[2][4] = {};
#pragma unroll
    for (int s = 0; s < 9; ++s) {
#pragma unroll
      for (int t = 0; t < 4; ++t) {
        const u32x4 x = *(const u32x4*)(sm + (t * 18 + kh * 9 + s) * 1024 + lane * 16);
        acc[0][t] = mfma(a[s][0], x, acc[0][t]);
        acc[1][t] = mfma(a[s][1], x, acc[1][t]);
      }
    }
    BAR();
    {
      char* e = sm + wv * 4096 + lane * 16;
#pragma unroll
      for (int n = 0; n < 2; ++n) {
        *(f32x4*)(e + (n * 2 + 0) * 1024) = kh ? acc[n][0] : acc[n][2];
        *(f32x4*)(e + (n * 2 + 1) * 1024) = kh ? acc[n][1] : acc[n][3];
      }
    }
    BAR();
    {
      const char* e = sm + (kh ? wv - 3 : wv + 3) * 4096 + lane * 16;
      const rsrc_t rx = mk(ws + O_X1, ACT), rs2 = mk(ws + O_ST2, 18 * S_ * 8);
#pragma unroll
      for (int u = 0; u < 2; ++u) {
        const int mt = Mt * 4 + 2 * kh + u;
        const f32x4 v0 = (kh ? acc[0][2 + u] : acc[0][u]) + *(const f32x4*)(e + u * 1024);
        const f32x4 v1 = (kh ? acc[1][2 + u] : acc[1][u]) + *(const f32x4*)(e + (2 + u) * 1024);
        const f32x2 st = __builtin_bit_cast(f32x2, s1[u]);
        u32x4 o;
        float su = 0.f, sq = 0.f;
#pragma unroll
        for (int p = 0; p < 4; ++p) {
          const unsigned d = pk2(blo(xq[u][p]) - st[0], bhi(xq[u][p]) - st[0]);
          const unsigned xn = pk2(blo(d) * st[1], bhi(d) * st[1]);
          const unsigned hs = pk2(v0[p], v1[p]);
          o[p] = pk2(blo(xn) + blo(hs), bhi(xn) + bhi(hs));
          const float f0 = blo(o[p]), f1 = bhi(o[p]);
          su += f0 + f1;
          sq = __builtin_fmaf(f0, f0, __builtin_fmaf(f1, f1, sq));
        }
        st128(o, rx, lane * 16, (mt * 18 + P) * 1024);
        su += __shfl_xor(su, 16);
        sq += __shfl_xor(sq, 16);
        su += __shfl_xor(su, 32);
        sq += __shfl_xor(sq, 32);
        if (g == 0) st64(__builtin_bit_cast(u32x2, f32x2{su, sq}), rs2, r * 8, (P * S_ + mt * 16) * 8);
      }
    }
    return;
  }
  b -= K2_GEMM;
  if (b < K2_CONV) {
    if (wv >= 4) {
      const int PP = b >> 1, P0 = (b & 1) * 9 + (wv - 4) * 5, np = 9 - wv;
      const int sp = PP / 18, ks = PP % 18;
      const rsrc_t rw = mk(w4, WBG), ro = mk(ws + O_W4, WBG);
      unsigned w[5][8];
#pragma unroll
      for (int j = 0; j < 8; ++j) {
        const int T = 2 * PP + (j >> 2), f0 = (T >> 3) * 128 + (T & 7) + (j & 3) * 8;
#pragma unroll
        for (int h = 0; h < 5; ++h)
          w[h][j] = ld32(rw, h < np ? g * 32 * 1152 + r * 4 : 0x80000000u, f0 * 1152 + (P0 + h) * 64);
      }
      FENCE();
      __builtin_amdgcn_s_barrier();
#pragma unroll
      for (int h = 0; h < 5; ++h) {
        if (h < np) {
          u32x4 a0, a1;
#pragma unroll
          for (int p = 0; p < 4; ++p) {
            a0[p] = plo(w[h][2 * p], w[h][2 * p + 1]);
            a1[p] = phi(w[h][2 * p], w[h][2 * p + 1]);
          }
          const int T0 = 2 * (P0 + h), T1 = T0 + 1;
          st128(a0, ro, lane * 16, ((sp * 12 + T0 / 3) * 54 + ks * 3 + T0 % 3) * 1024);
          st128(a1, ro, lane * 16, ((sp * 12 + T1 / 3) * 54 + ks * 3 + T1 % 3) * 1024);
        }
      }
      return;
    }
    const int c1 = wv >> 1, hf = wv & 1, ch = 2 * b + c1;
    char* cb = sm + c1 * CV_CH;
    const rsrc_t rkv = mk(ws + O_KV, ACT), rk = mk(ssk, ACT);
#pragma unroll
    for (int n = 0; n < 2; ++n) {
      const int c = (hf * 2 + n) * 64 + lane;
      glds16(rkv, cb + CV_KV + 512 + (hf * 2 + n) * 1024, (c >> 2) * 128 + (c & 3) * 16, b * 8192 + c1 * 64);
    }
    u32x4 kc[2];
    unsigned kn[2];
#pragma unroll
    for (int n = 0; n < 2; ++n) {
      const int c = (hf * 2 + n) * 64 + lane;
      kc[n] = ld128(rk, ch * 4096 + c * 16, 0);
      kn[n] = ld32(rk, ch * 4096 + c * 16 + 16, 0);
    }
    const unsigned dd = ld32(mk(ssd, D_ * 2), b * 4, 0);
    if (hf == 0) {
      if (lane < 32) *(u32x4*)(cb + CV_KV + lane * 16) = u32x4{0, 0, 0, 0};
      if (lane < 2) *(u32x4*)(cb + CV_E + lane * 16) = u32x4{0, 0, 0, 0};
      if (lane == 0) {
        *(u32x4*)(cb + CV_O) = u32x4{0, 0, 0, 0};
        *(u32x4*)(cb + CV_O + 16) = u32x4{0, 0, 0, kc[0][0] << 16};
      }
    }
#pragma unroll
    for (int n = 0; n < 2; ++n) {
      const int c = (hf * 2 + n) * 64 + lane;
      *(u32x4*)(cb + CV_E + 32 + c * 16) = kc[n];
      const u32x4 o = {__builtin_amdgcn_alignbyte(kc[n][1], kc[n][0], 2), __builtin_amdgcn_alignbyte(kc[n][2], kc[n][1], 2),
                       __builtin_amdgcn_alignbyte(kc[n][3], kc[n][2], 2), __builtin_amdgcn_alignbyte(kn[n], kc[n][3], 2)};
      *(u32x4*)(cb + CV_O + 32 + c * 16) = o;
    }
    WAITVM(0);
    BAR();
    f32x4 acc[8] = {};
    const unsigned* ka = (const unsigned*)(cb + ((r & 1) ? CV_E : CV_O)) + ((r + 1) >> 1) + 4 * g + hf * 64;
    const char* kb = cb + CV_KV + 512 + (2 * r + 1 - g - hf * 16) * 16;
#pragma unroll
    for (int u = 0; u < 4; ++u) {
      u32x4 A[8], B[8];
#pragma unroll
      for (int i = 0; i < 8; ++i) {
        const unsigned* p = ka + u * 16 + i * 128;
        A[i] = u32x4{p[0], p[1], p[2], p[3]};
        B[i] = *(const u32x4*)(kb + (i * 32 - u * 4) * 16);
      }
#pragma unroll
      for (int i = 0; i < 8; ++i)
#pragma unroll
        for (int m = 0; m + i < 8; ++m) acc[i + m] = mfma(A[i], B[m], acc[i + m]);
    }
#pragma unroll
    for (int n = 0; n < 8; ++n) *(f32x4*)(sm + CV_Y + wv * 8192 + n * 1024 + r * 64 + g * 16) = acc[n];
    BAR();
    const rsrc_t rz = mk(ws + O_Z, ACT);
#pragma unroll
    for (int n = 0; n < 2; ++n) {
      const int id = n * 256 + tid, sb = id >> 3, cc = (id >> 2) & 1, c = sb * 4 + (id & 3);
      const char* y0 = sm + CV_Y + cc * 16384 + c * 32;
      const f32x4 ya = *(const f32x4*)y0 + *(const f32x4*)(y0 + 8192);
      const f32x4 yb = *(const f32x4*)(y0 + 16) + *(const f32x4*)(y0 + 8192 + 16);
      const u32x4 kv = *(const u32x4*)(sm + cc * CV_CH + CV_KV + 512 + c * 16);
      const float dv = cc ? bhi(dd) : blo(dd);
      u32x4 o;
#pragma unroll
      for (int p = 0; p < 4; ++p) {
        const unsigned yy = p < 2 ? pk2(ya[2 * p], ya[2 * p + 1]) : pk2(yb[2 * p - 4], yb[2 * p - 3]);
        const unsigned kd = pk2(bhi(kv[3 - p]) * dv, blo(kv[3 - p]) * dv);
        o[p] = pk2(blo(yy) + blo(kd), bhi(yy) + bhi(kd));
      }
      st128(o, rz, sb * (D_ * 64) + (id & 7) * 16, b * 128);
    }
    return;
  }
  b -= K2_CONV;
  {
    const int f = b * 384 + tid;
    const rsrc_t rc = mk(ws + O_CSP, 18 * F_ * 4);
    float s = 0.f;
#pragma unroll
    for (int j = 0; j < 18; ++j) s += __builtin_bit_cast(float, ld32(rc, f * 4, j * F_ * 4));
    ((float*)(ws + O_CS))[f] = s;
  }
}

constexpr int L3_X = 0, L3_H = 73728, L3_CS = 147456, L3_ST = L3_CS + 3072, L3_FL = L3_ST + 512, L3_PF = L3_FL + 256,
              LDS3 = L3_PF + 256;
#define RING 6

template <int OFF> DEVI void wait_flag(unsigned zero) {
  unsigned tv, ts;
  asm volatile(
      "1:\n\t"
      "ds_read_b32 %0, %2 offset:%3\n\t"
      "s_waitcnt lgkmcnt(0)\n\t"
      "v_readfirstlane_b32 %1, %0\n\t"
      "s_cmp_eq_u32 %1, 0x01010101\n\t"
      "s_cbranch_scc1 2f\n\t"
      "s_sleep 2\n\t"
      "s_branch 1b\n\t"
      "2:"
      : "=&v"(tv), "=&s"(ts)
      : "v"(zero), "n"(OFF)
      : "memory", "scc");
}
template <int P0, int P1, int P2> DEVI void prio_by_group(int grp) {
  asm volatile(
      "s_setprio %1\n\t"
      "s_cmp_eq_u32 %0, 0\n\t"
      "s_cbranch_scc1 1f\n\t"
      "s_setprio %2\n\t"
      "s_cmp_eq_u32 %0, 1\n\t"
      "s_cbranch_scc1 1f\n\t"
      "s_setprio %3\n\t"
      "1:" ::"s"(grp), "n"(P0), "n"(P1), "n"(P2)
      : "scc");
}

__global__ __launch_bounds__(768, 3) void k3(char* __restrict__ ws) {
  extern __shared__ __attribute__((aligned(16))) char sm[];
  const int tid = threadIdx.x, lane = tid & 63;
  const int wv = __builtin_amdgcn_readfirstlane(tid >> 6);
  const int r = lane & 15, g = lane >> 4;
  const int sp = blockIdx.x & 7, Mt = blockIdx.x >> 3, grp = wv >> 2;
  const rsrc_t rw = mk(ws + O_W3, 2 * WBG), rx = mk(ws + O_X1, ACT);
  const unsigned l16 = lane * 16;
  const unsigned wb = (sp * 12 + wv) * 55296;
#pragma unroll
  for (int n = 0; n < 6; ++n) {
    const int pc = n * 12 + wv;
    glds16(rx, sm + L3_X + pc * 1024, l16, Mt * 73728 + ((pc & 3) * 18 + (pc >> 2)) * 1024);
  }
  if (wv >= 4 && wv < 10) {
    const int n = wv - 4, ln = (n % 3) * 64 + lane;
    glds4(rw, sm + L3_PF, ln < 162 ? ln * 128 : 0x80000000u, (n / 3) * WBG + sp * 663552 + Mt * 20736);
  }
  if (wv == 10) {
    const rsrc_t rc = mk(ws + O_CS, 20480);
#pragma unroll
    for (int n = 0; n < 3; ++n) glds16(rc, sm + L3_CS + n * 1024, l16, sp * 2304 + n * 1024);
  }
  if (wv == 11) {
    const rsrc_t rs2 = mk(ws + O_ST2, 18 * S_ * 8);
    float su = 0.f, sq = 0.f;
#pragma unroll
    for (int j = 0; j < 18; ++j) {
      const f32x2 v = __builtin_bit_cast(f32x2, ld64(rs2, lane * 8, (j * S_ + Mt * 64) * 8));
      su += v[0];
      sq += v[1];
    }
    const float mean = su * (1.0f / D_);
    const float var = fmaxf(sq * (1.0f / D_) - mean * mean, 0.0f);
    const float rs = 1.0f / rbf(__builtin_sqrtf(rbf(rbf(var) + 1e-5f)));
    *(f32x2*)(sm + L3_ST + lane * 8) = f32x2{rs, -mean * rs};
    if (lane < 4) *(unsigned*)(sm + L3_FL + lane * 4) = 0;
  }
  FENCE();
  u32x4 a[RING][3];
#pragma unroll
  for (int i = 0; i < RING - 1; ++i)
#pragma unroll
    for (int t = 0; t < 3; ++t) a[i][t] = ld128(rw, l16, wb + (i * 3 + t) * 1024);
  FENCE();
  asm volatile("s_waitcnt vmcnt(%0)" ::"n"(3 * (RING - 1)) : "memory");
  BAR();
  prio_by_group<3, 2, 1>(grp);

  f32x4 acc[3][4];
  u32x4 x[2][4];
  unsigned lb = l16;
#pragma unroll
  for (int t = 0; t < 4; ++t) x[0][t] = *(const u32x4*)(sm + L3_X + t * 1024 + l16);
  sfor<0, 36>([&](auto ic) __attribute__((always_inline)) {
    constexpr int i = decltype(ic)::v;
    constexpr int nx = i + RING - 1;
    if constexpr (nx < 36) {
#pragma unroll
      for (int t = 0; t < 3; ++t)
        a[nx % RING][t] = ld128(rw, l16, wb + (nx < 18 ? nx * 3 + t : (nx - 18) * 3 + t) * 1024 + (nx < 18 ? 0 : WBG));
    }
    if constexpr (i == 18) {
      __builtin_amdgcn_s_setprio(0);
      f32x2 st[4];
#pragma unroll
      for (int m = 0; m < 4; ++m) st[m] = *(const f32x2*)(sm + L3_ST + (m * 16 + r) * 8);
#pragma unroll
      for (int t = 0; t < 3; ++t) {
        const int tl = wv * 3 + t;
        const f32x4 c = *(const f32x4*)(sm + L3_CS + tl * 64 + g * 16);
        char* hp = sm + L3_H + (tl >> 1) * 4096 + (tl & 1) * 8 + l16;
#pragma unroll
        for (int m = 0; m < 4; ++m) {
          float h[4];
#pragma unroll
          for (int p = 0; p < 4; ++p) h[p] = gelu_t(__builtin_fmaf(acc[t][m][p], st[m][0], st[m][1] * c[p]));
          *(u32x2*)(hp + m * 1024) = u32x2{pk2(h[0], h[1]), pk2(h[2], h[3])};
        }
      }
      asm volatile("s_waitcnt lgkmcnt(0)" ::: "memory");
      sm[L3_FL + wv] = 1;
      wait_flag<0>(L3_FL);
#pragma unroll
      for (int t = 0; t < 4; ++t) x[0][t] = *(const u32x4*)(sm + (24 + t) * 1024 + lb);
    }
    if constexpr (i == 23) wait_flag<4>(L3_FL);
    if constexpr (i == 29) {
      wait_flag<8>(L3_FL);
      prio_by_group<3, 2, 0>(grp);
    }
    if constexpr (i == 11 || i == 23) asm volatile("v_add_u32 %0, 0xc000, %0" : "+v"(lb));
    if constexpr (i != 17 && i != 35) {
#pragma unroll
      for (int t = 0; t < 4; ++t) x[(i + 1) & 1][t] = *(const u32x4*)(sm + (((i + 1) % 12) * 4 + t) * 1024 + lb);
    }
    FENCE();
#pragma unroll
    for (int m = 0; m < 4; ++m)
#pragma unroll
      for (int t = 0; t < 3; ++t)
        acc[t][m] = mfma(a[i % RING][t], x[i & 1][m], (i == 0 || i == 18) ? f32x4{0, 0, 0, 0} : acc[t][m]);
    FENCE();
  });
  const rsrc_t rp = mk(ws + O_PART, 8 * ACT);
#pragma unroll
  for (int t = 0; t < 3; ++t)
#pragma unroll
    for (int h = 0; h < 2; ++h) {
      const f32x4 v0 = acc[t][2 * h], v1 = acc[t][2 * h + 1];
      const u32x4 o = {pk2(v0[0], v0[1]), pk2(v0[2], v0[3]), pk2(v1[0], v1[1]), pk2(v1[2], v1[3])};
      st128a<ST_AUX3>(o, rp, l16, sp * ACT + Mt * 73728 + ((wv * 3 + t) * 2 + h) * 1024);
    }
}

__global__ __launch_bounds__(256) void k4(const void* __restrict__ q, const char* __restrict__ ws, void* __restrict__ out) {
  __shared__ __attribute__((aligned(16))) char sm[2 * 8704 + 4096];
  const int tid = threadIdx.x, lane = tid & 63;
  const int wv = __builtin_amdgcn_readfirstlane(tid >> 6);
  const int r = lane & 15, g = lane >> 4;
  const int G = blockIdx.x % 9, sb = blockIdx.x / 9, Mt = sb >> 1, mp = sb & 1;
  const int pl = wv & 1, sh = wv >> 1, P = 2 * G + pl;
  const rsrc_t rp = mk(ws + O_PART, 8 * ACT), rx = mk(ws + O_X1, ACT), rz = mk(ws + O_Z, ACT), rq = mk(q, ACT),
               rs1 = mk(ws + O_ST1, S_ * 8), ro = mk(out, ACT);
  u32x4 pp[4][2];
#pragma unroll
  for (int s = 0; s < 4; ++s)
#pragma unroll
    for (int t = 0; t < 2; ++t)
      pp[s][t] = ld128(rp, lane * 16, (sh * 4 + s) * ACT + Mt * 73728 + ((2 * P + t) * 2 + mp) * 1024);
  const u32x4 x1 = ld128(rx, lane * 16, ((sb * 2 + sh) * 18 + P) * 1024);
  const u32x4 zz = ld128(rz, tid * 16, sb * (D_ * 64) + G * 4096);
  const int row = tid >> 3, c8 = tid & 7;
  const unsigned ooff = row * 1152 + c8 * 16, osf = sb * 32 * 1152 + G * 128;
  const u32x4 xq = ld128(rq, ooff, osf);
  const f32x2 st = __builtin_bit_cast(f32x2, ld64(rs1, row * 8, sb * 256));
  FENCE();
  *(u32x4*)(sm + 17408 + (tid ^ ((tid >> 5) & 3)) * 16) = zz;
#pragma unroll
  for (int i = 0; i < 2; ++i) {
    float v[8];
#pragma unroll
    for (int p = 0; p < 4; ++p)
#pragma unroll
      for (int t = 0; t < 2; ++t) {
        float a = 0.f;
#pragma unroll
        for (int s = 0; s < 4; ++s) a += (p & 1) ? bhi(pp[s][t][2 * i + (p >> 1)]) : blo(pp[s][t][2 * i + (p >> 1)]);
        const float xv = t ? bhi(x1[p]) : blo(x1[p]);
        v[2 * p + t] = a + (i == sh ? xv : 0.f);
      }
    char* e = sm + sh * 8704 + (i * 16 + r) * 272 + pl * 128 + g * 32;
    *(f32x4*)e = f32x4{v[0], v[1], v[2], v[3]};
    *(f32x4*)(e + 16) = f32x4{v[4], v[5], v[6], v[7]};
  }
  BAR();
  const char* e = sm + row * 272 + c8 * 32;
  const f32x4 b0 = *(const f32x4*)e + *(const f32x4*)(e + 8704);
  const f32x4 b1 = *(const f32x4*)(e + 16) + *(const f32x4*)(e + 8704 + 16);
  const char* zp = sm + 17408 + c8 * 512 + (((row >> 3) ^ (c8 & 3)) * 16) + (row & 7) * 2;
  float o[8];
#pragma unroll
  for (int j = 0; j < 8; ++j) {
    const float bo = rbf(j < 4 ? b0[j] : b1[j - 4]);
    const float gt = rbf(bo * __builtin_amdgcn_rcpf(1.0f + __builtin_amdgcn_exp2f(bo * -1.442695041f)));
    const float z = blo(*(const unsigned short*)(zp + j * 64));
    const float xv = (j & 1) ? bhi(xq[j >> 1]) : blo(xq[j >> 1]);
    const float xn = rbf(rbf(xv - st[0]) * st[1]);
    o[j] = xv + rbf(rbf(xn * z) * gt);
  }
  st128a<0>(u32x4{pk2(o[0], o[1]), pk2(o[2], o[3]), pk2(o[4], o[5]), pk2(o[6], o[7])}, ro, ooff, osf);
}

extern "C" void kernel_launch(void* const* d_in, const int* in_sizes, int n_in, void* d_out, int out_size, void* d_ws,
                              size_t ws_size, hipStream_t stream) {
  (void)in_sizes; (void)n_in; (void)out_size; (void)ws_size;
  char* ws = (char*)d_ws;
  (void)hipFuncSetAttribute((const void*)k1, hipFuncAttributeMaxDynamicSharedMemorySize, LDS12);
  (void)hipFuncSetAttribute((const void*)k2, hipFuncAttributeMaxDynamicSharedMemorySize, LDS12);
  (void)hipFuncSetAttribute((const void*)k3, hipFuncAttributeMaxDynamicSharedMemorySize, LDS3);
  k1<<<K1_GEMM + K1_KV + K1_W3 + K1_W2, 384, LDS12, stream>>>(d_in[0], d_in[1], d_in[2], d_in[3], d_in[7], d_in[8], d_in[9], ws);
  k2<<<K2_GEMM + K2_CONV + K2_CS, 384, LDS12, stream>>>(d_in[0], d_in[4], d_in[5], d_in[10], ws);
  k3<<<256, 768, LDS3, stream>>>(ws);
  k4<<<576, 256, 0, stream>>>(d_in[0], ws, d_out);
}
```

```cpp
#include <hip/hip_runtime.h>
#include <hip/hip_bf16.h>
#include <stdint.h>

typedef __attribute__((ext_vector_type(8))) __bf16 bf16x8;
typedef __attribute__((ext_vector_type(2))) __bf16 bf16x2;
typedef __attribute__((ext_vector_type(4))) float f32x4;
typedef __attribute__((ext_vector_type(2))) float f32x2;
typedef __attribute__((ext_vector_type(4))) unsigned u32x4;
typedef __attribute__((ext_vector_type(2))) unsigned u32x2;
typedef __amdgpu_buffer_rsrc_t rsrc_t;
typedef __attribute__((address_space(3))) void* lds_ptr;

#define DEVI __device__ __forceinline__
#if defined(__HIP_DEVICE_COMPILE__) && !defined(PKOK)
#pragma clang attribute push(__attribute__((target("no-packed-fp32-ops"))), apply_to = function)
#endif

constexpr int S_ = 2048, D_ = 576, F_ = 4608;
constexpr int ACT = S_ * D_ * 2;
constexpr int WSM = D_ * D_ * 2;
constexpr int WBG = D_ * F_ * 2;
constexpr size_t O_HF = 0;
constexpr size_t O_KV = O_HF + ACT;
constexpr size_t O_X1 = O_KV + ACT;
constexpr size_t O_Z = O_X1 + ACT;
constexpr size_t O_W2 = O_Z + ACT;
constexpr size_t O_W3 = O_W2 + WSM;
constexpr size_t O_W4 = O_W3 + WBG;
constexpr size_t O_CSP = O_W4 + WBG;
constexpr size_t O_CS = O_CSP + 18 * F_ * 4;
constexpr size_t O_ST1 = O_CS + 20480;
constexpr size_t O_ST2 = O_ST1 + S_ * 8;
constexpr size_t O_PART = O_ST2 + 18 * S_ * 8;

#define ST_AUX 16
#define ST_AUX3 16

DEVI rsrc_t mk(const void* p, int bytes) {
  return __builtin_amdgcn_make_buffer_rsrc((void*)p, (short)0, bytes, 0x00020000);
}
DEVI unsigned ld32(rsrc_t r, unsigned v, unsigned s) { return __builtin_amdgcn_raw_buffer_load_b32(r, v, s, 0); }
DEVI u32x2 ld64(rsrc_t r, unsigned v, unsigned s) { return __builtin_amdgcn_raw_buffer_load_b64(r, v, s, 0); }
DEVI u32x4 ld128(rsrc_t r, unsigned v, unsigned s) { return __builtin_amdgcn_raw_buffer_load_b128(r, v, s, 0); }
template <int AUX> DEVI void st128a(u32x4 d, rsrc_t r, unsigned v, unsigned s) {
  __builtin_amdgcn_raw_buffer_store_b128(d, r, v + s, 0, AUX);
}
DEVI void st128(u32x4 d, rsrc_t r, unsigned v, unsigned s) { st128a<ST_AUX>(d, r, v, s); }
DEVI void st64(u32x2 d, rsrc_t r, unsigned v, unsigned s) { __builtin_amdgcn_raw_buffer_store_b64(d, r, v, s, ST_AUX); }
DEVI void glds16(rsrc_t r, char* lds, unsigned v, unsigned s) {
  __builtin_amdgcn_raw_ptr_buffer_load_lds(r, (lds_ptr)lds, 16, v, s, 0, 0);
}
DEVI void glds4(rsrc_t r, char* lds, unsigned v, unsigned s) {
  __builtin_amdgcn_raw_ptr_buffer_load_lds(r, (lds_ptr)lds, 4, v, s, 0, 0);
}
DEVI unsigned pk2(float a, float b) {
  bf16x2 v = {(__bf16)a, (__bf16)b};
  return __builtin_bit_cast(unsigned, v);
}
DEVI float blo(unsigned u) { return __builtin_bit_cast(float, u << 16); }
DEVI float bhi(unsigned u) { return __builtin_bit_cast(float, u & 0xffff0000u); }
DEVI float rbf(float a) { return (float)(__bf16)a; }
DEVI f32x4 mfma(u32x4 a, u32x4 b, f32x4 c) {
  return __builtin_amdgcn_mfma_f32_16x16x32_bf16(__builtin_bit_cast(bf16x8, a), __builtin_bit_cast(bf16x8, b), c, 0, 0, 0);
}
DEVI unsigned plo(unsigned a, unsigned b) { return __builtin_amdgcn_perm(b, a, 0x05040100u); }
DEVI unsigned phi(unsigned a, unsigned b) { return __builtin_amdgcn_perm(b, a, 0x07060302u); }
DEVI float shx(float v, int src) {
  return __builtin_bit_cast(float, __builtin_amdgcn_ds_bpermute(src * 4, __builtin_bit_cast(int, v)));
}
DEVI float gelu_t(float x) {
  const float c0 = -2.302208198f, c1 = -0.1029432397f;
  float e = __builtin_amdgcn_exp2f(x * __builtin_fmaf(x * x, c1, c0));
  return x * __builtin_amdgcn_rcpf(1.0f + e);
}
#define WAITVM(n) asm volatile("s_waitcnt vmcnt(" #n ")" ::: "memory")
#define BAR()                                          \
  do {                                                 \
    asm volatile("s_waitcnt lgkmcnt(0)" ::: "memory"); \
    __builtin_amdgcn_s_barrier();                      \
    asm volatile("" ::: "memory");                     \
  } while (0)
#define FENCE() __builtin_amdgcn_sched_barrier(0)

#define TS_DECL
#define TS(i)
#define TS_END(kn, wsp)

template <int V> struct IC { static constexpr int v = V; };
template <int I, int N, class Fn> DEVI void sfor(Fn&& f) {
  if constexpr (I < N) {
    f(IC<I>{});
    sfor<I + 1, N>(f);
  }
}

constexpr unsigned ONES = 0x3f803f80u;
constexpr int LDS12 = 73728;

template <int OFF> DEVI void wait_flag(unsigned zero) {
  unsigned tv, ts;
  asm volatile(
      "1:\n\t"
      "ds_read_b32 %0, %2 offset:%3\n\t"
      "s_waitcnt lgkmcnt(0)\n\t"
      "v_readfirstlane_b32 %1, %0\n\t"
      "s_cmp_eq_u32 %1, 0x01010101\n\t"
      "s_cbranch_scc1 2f\n\t"
      "s_sleep 2\n\t"
      "s_branch 1b\n\t"
      "2:"
      : "=&v"(tv), "=&s"(ts)
      : "v"(zero), "n"(OFF)
      : "memory", "scc");
}
constexpr int T1R = 74, C1X = 75 * 384, C1 = C1X + 192 * 128, L1_FL = 3 * C1, LDS1 = L1_FL + 64;
constexpr int U_W3 = 648, U_KV = 576, U_W2 = 162;
typedef __attribute__((ext_vector_type(4))) short s16x4;
DEVI u32x2 ldtr(const char* p) {
  return __builtin_bit_cast(u32x2, __builtin_amdgcn_ds_read_tr16_b64_v4i16((__attribute__((address_space(3))) s16x4*)p));
}
DEVI float bc15(float v) {
  return __builtin_bit_cast(float, __builtin_amdgcn_update_dpp(0, __builtin_bit_cast(int, v), 0x15f, 0xf, 0xf, false));
}
#define KEEP(x) asm volatile("" : "+v"(x))
#define PASSES int pass = 0; asm volatile("" : "+s"(pass)); _Pragma("unroll 1") for (; pass < 2; ++pass)

__global__ __launch_bounds__(896, 4) void k1(const void* __restrict__ q, const void* __restrict__ kin,
                                             const void* __restrict__ vin, const void* __restrict__ shk,
                                             const void* __restrict__ w1, const void* __restrict__ w2,
                                             const void* __restrict__ w3, char* __restrict__ ws) {
  extern __shared__ __attribute__((aligned(16))) char sm[];
  TS_DECL;
  const int tid = threadIdx.x, lane = tid & 63;
  const int wv = __builtin_amdgcn_readfirstlane(tid >> 6);
  const int r = lane & 15, g = lane >> 4;
  const int b = blockIdx.x, tile = (b & 7) * 32 + (b >> 3), Mt = tile / 9, Nt = tile - Mt * 9;
  const u32x4 ones = {ONES, ONES, ONES, ONES};
  if (wv < 3 && lane < 24) *(u32x4*)(sm + wv * C1 + T1R * 384 + lane * 16) = ones;
  if (wv == 3 && lane < 4) *(unsigned*)(sm + L1_FL + lane * 4) = 0;
  BAR();
  if (wv < 8 && tile >= 252) return;
  if (wv < 4) {
    const int pr = wv & 1, mh = wv >> 1, P = Nt * 2 + pr;
    const unsigned wa = C1X + (8 * g + (r >> 2)) * 128 + (((pr * 4 + (r & 3)) ^ ((r >> 3) << 2)) * 16);
    unsigned xa[3][2];
#pragma unroll
    for (int m = 0; m < 3; ++m) {
      const int mt = mh * 3 + m > 4 ? 4 : mh * 3 + m, row = r == 15 ? T1R : mt * 15 + r, sw = (row >> 1) & 7;
      xa[m][0] = row * 384 + ((g ^ sw) * 16);
      xa[m][1] = row * 384 + (((4 + g) ^ sw) * 16);
    }
    const rsrc_t rh = mk(ws + O_HF, ACT), rs1 = mk(ws + O_ST1, S_ * 8);
    const int grp = (P * 4 + g) / 18;
    const int sh = grp == 0 ? 1 : grp == 1 ? -1 : grp == 2 ? 2 : -2;
    PASSES {
      f32x4 acc[3][2] = {}, gm[3] = {};
#pragma unroll 1
      for (int c = pass ? 0 : 2; c < 3; ++c) {
        if (pass) wait_flag<0>(L1_FL + c * 4);
        if (c == 0) TS(2);
        if (c == 2) TS(3);
        const char* cb = sm + c * C1;
        u32x4 a[2][2], x[2][3];
#pragma unroll
        for (int s = 0; s < 7; ++s) {
          if (s < 6) {
#pragma unroll
            for (int m = 0; m < 3; ++m) x[s & 1][m] = *(const u32x4*)(cb + xa[m][s & 1] + (s >> 1) * 128);
#pragma unroll
            for (int t = 0; t < 2; ++t) {
              const u32x2 lo = ldtr(cb + wa + s * 4096 + t * 8), hi = ldtr(cb + wa + s * 4096 + 512 + t * 8);
              a[s & 1][t] = u32x4{lo[0], lo[1], hi[0], hi[1]};
            }
          }
          FENCE();
          if (s > 0) {
            const int u = (s - 1) & 1;
#pragma unroll
            for (int m = 0; m < 3; ++m) gm[m] = mfma(x[u][m], x[u][m], gm[m]);
#pragma unroll
            for (int t = 0; t < 2; ++t)
#pragma unroll
              for (int m = 0; m < 3; ++m) acc[m][t] = mfma(a[u][t], x[u][m], acc[m][t]);
          }
          FENCE();
        }
      }
      TS(4);
#pragma unroll
      for (int m = 0; m < 3; ++m) {
        if (m == 2 && mh) break;
        const float su = shx(gm[m][3], 48 + r);
        const float d0 = (r & 1) ? gm[m][1] : gm[m][0], d1 = (r & 1) ? gm[m][3] : gm[m][2];
        const float sq = shx((r & 2) ? d1 : d0, r + 16 * (r >> 2));
        const float mean = su * (1.0f / D_);
        const float var = __builtin_fmaxf(sq * (1.0f / D_) - mean * mean, 0.0f);
        const float r1 = __builtin_amdgcn_rcpf(rbf(__builtin_amdgcn_sqrtf(rbf(rbf(var) + 1e-5f))));
        const float sc = r1 * __builtin_amdgcn_rsqf(var * r1 * r1 + 1e-5f);
        const float nm = -mean * sc;
        float h[8];
#pragma unroll
        for (int p = 0; p < 8; ++p) {
          const float v = acc[m][p >> 2][p & 3];
          h[p] = gelu_t(__builtin_fmaf(v, sc, nm * bc15(v)));
        }
        u32x4 o = {pk2(h[0], h[1]), pk2(h[2], h[3]), pk2(h[4], h[5]), pk2(h[6], h[7])};
        const int lr = (mh * 3 + m) * 15 + r, row = Mt * T1R + lr;
        const bool ok = r < 15 && lr < T1R && row < S_;
        const int dr = row + sh, dw = dr & (S_ - 1);
        if (dr != dw) o = u32x4{0, 0, 0, 0};
        unsigned ho = ok ? (dw >> 4) * 18432 + (g * 16 + (dw & 15)) * 16 + P * 1024 : 0x80000000u;
        u32x2 sv = __builtin_bit_cast(u32x2, f32x2{rbf(mean), r1});
        KEEP(o);
        KEEP(ho);
        KEEP(sv);
        if (pass) {
          st128(o, rh, ho, 0);
          if (P == 0) st64(sv, rs1, ok && g == 0 ? row * 8 : 0x80000000u, 0);
        }
      }
      if (!pass) TS(5);
    }
    TS_END(0, ws);
    return;
  }
  if (wv < 8) {
    const int w = wv - 4;
    const rsrc_t rq = mk(q, ACT), rw = mk(w1, WSM);
    unsigned xo[7];
#pragma unroll
    for (int j = 0; j < 7; ++j) {
      const int L = (j * 4 + w) * 64 + lane, row = L / 24;
      xo[j] = row * 1152 + (((L - row * 24) ^ ((row >> 1) & 7)) * 16);
    }
    const unsigned wo = (w * 8 + (lane >> 3)) * 1152 + (((lane & 7) ^ ((lane >> 2) & 4)) * 16);
#pragma unroll 1
    for (int c = 0; c < 3; ++c) {
      char* cb = sm + c * C1 + w * 1024;
#pragma unroll
      for (int j = 0; j < 7; ++j) {
        if (j < 6 || w < 3 || lane < 48) glds16(rq, cb + j * 4096, xo[j], Mt * (T1R * 1152) + c * 384);
        if (j < 6) glds16(rw, cb + C1X + j * 4096, wo, (c * 192 + j * 32) * 1152 + Nt * 128);
      }
      if (c) {
        WAITVM(13);
        sm[L1_FL + (c - 1) * 4 + w] = 1;
      }
    }
    TS(2);
    WAITVM(0);
    sm[L1_FL + 8 + w] = 1;
    TS_END(0, ws);
    return;
  }
  int un = (wv - 8) * 256 + b;
  if (un < U_W3) {
    const int G = un % 36, ks = un / 36;
    const rsrc_t rw = mk(w3, WBG), ro = mk(ws + O_W3, WBG), rc = mk(ws + O_CSP, 18 * F_ * 4);
    PASSES {
      u32x4 w[8] = {};
      if (pass) {
#pragma unroll
        for (int j = 0; j < 8; ++j) w[j] = ld128(rw, g * 8 * 9216 + r * 16, (ks * 32 + j) * 9216 + G * 256);
      }
      f32x4 mine = {};
#pragma unroll
      for (int t = 0; t < 8; ++t) {
        u32x4 a;
#pragma unroll
        for (int p = 0; p < 4; ++p)
          a[p] = (t & 1) ? phi(w[2 * p][t >> 1], w[2 * p + 1][t >> 1]) : plo(w[2 * p][t >> 1], w[2 * p + 1][t >> 1]);
        const int T = G * 8 + t;
        unsigned o = lane * 16 + ((T / 3) * 54 + ks * 3 + T % 3) * 1024;
        KEEP(a);
        KEEP(o);
        if (pass) st128(a, ro, o, 0);
        const f32x4 c = mfma(a, ones, f32x4{0, 0, 0, 0});
        if (r == t) mine = c;
      }
      KEEP(mine);
      if (pass) st128(__builtin_bit_cast(u32x4, mine), rc, r < 8 ? r * 64 + g * 16 : 0x80000000u, ks * F_ * 4 + G * 512);
      else TS(2);
    }
    TS_END(0, ws);
    return;
  }
  un -= U_W3;
  if (un < U_KV) {
    const int G = un % 9, sb = un / 9;
    const rsrc_t rk = mk(kin, ACT), rv = mk(vin, ACT), rsk = mk(shk, D_ * 8), ro = mk(ws + O_KV, ACT);
    const int row0 = sb * 32 + g * 8;
    const unsigned off = row0 * 1152 + G * 128 + r * 8;
    const int src = ((lane & 3) * 16 + (lane >> 2)) * 4;
    PASSES {
      u32x2 kk[11] = {}, vv[8] = {};
      u32x4 s0 = {}, s1 = {};
      if (pass) {
        s0 = ld128(rsk, (G * 64 + r * 4) * 8, 0);
        s1 = ld128(rsk, (G * 64 + r * 4) * 8 + 16, 0);
#pragma unroll
        for (int j = 0; j < 3; ++j) kk[j] = ld64(rk, row0 == 0 ? 0x80000000u : off - (3 - j) * 1152, 0);
#pragma unroll
        for (int j = 0; j < 8; ++j) {
          kk[3 + j] = ld64(rk, off, j * 1152);
          vv[j] = ld64(rv, off, j * 1152);
        }
      }
#pragma unroll
      for (int t = 0; t < 4; ++t) {
        const unsigned sa = t < 2 ? s0[2 * t] : s1[2 * t - 4], sb2 = t < 2 ? s0[2 * t + 1] : s1[2 * t - 3];
        const float c0 = blo(sa), c1 = bhi(sa), c2 = blo(sb2), c3 = bhi(sb2);
        float kf[11], o[8];
#pragma unroll
        for (int j = 0; j < 11; ++j) kf[j] = (t & 1) ? bhi(kk[j][t >> 1]) : blo(kk[j][t >> 1]);
#pragma unroll
        for (int j = 0; j < 8; ++j) {
          const float ks = __builtin_fmaf(kf[j + 3], c0, __builtin_fmaf(kf[j + 2], c1, __builtin_fmaf(kf[j + 1], c2, kf[j] * c3)));
          o[j] = ks * ((t & 1) ? bhi(vv[j][t >> 1]) : blo(vv[j][t >> 1]));
        }
        u32x4 w;
#pragma unroll
        for (int p = 0; p < 4; ++p) w[p] = __builtin_amdgcn_ds_bpermute(src, pk2(o[7 - 2 * p], o[6 - 2 * p]));
        KEEP(w);
        if (pass) st128(w, ro, (lane >> 2) * 16384 + (lane & 3) * 16, (G * 32 + (t >> 1)) * 8192 + sb * 128 + (t & 1) * 64);
      }
      if (!pass) TS(2);
    }
    TS_END(0, ws);
    return;
  }
  un -= U_KV;
  if (un < U_W2) {
    const int G = un % 9, ks = un / 9;
    const rsrc_t rw = mk(w2, WSM), ro = mk(ws + O_W2, WSM);
    unsigned w[2][8];
#pragma unroll
    for (int j = 0; j < 8; ++j)
#pragma unroll
      for (int h = 0; h < 2; ++h) w[h][j] = ld32(rw, g * 9216 + r * 4, (ks * 32 + j) * 1152 + G * 128 + h * 64);
#pragma unroll
    for (int h = 0; h < 2; ++h) {
      u32x4 a0, a1;
#pragma unroll
      for (int p = 0; p < 4; ++p) {
        a0[p] = plo(w[h][2 * p], w[h][2 * p + 1]);
        a1[p] = phi(w[h][2 * p], w[h][2 * p + 1]);
      }
      st128(a0, ro, lane * 16, ((G * 2 + h) * 18 + ks) * 2048);
      st128(a1, ro, lane * 16, ((G * 2 + h) * 18 + ks) * 2048 + 1024);
    }
  }
  TS_END(0, ws);
}

constexpr int K2_GEMM = 192, K2_CONV = 288, K2_CS = 12;
constexpr int CV_KV = 0, CV_E = 4608, CV_O = CV_E + 4224, CV_CH = CV_O + 4224, CV_Y = 2 * CV_CH;

__global__ __launch_bounds__(384, 3) void k2(const void* __restrict__ q, const void* __restrict__ ssk,
                                             const void* __restrict__ ssd, const void* __restrict__ w4,
                                             char* __restrict__ ws) {
  extern __shared__ __attribute__((aligned(16))) char sm[];
  TS_DECL;
  const int tid = threadIdx.x, lane = tid & 63;
  const int wv = __builtin_amdgcn_readfirstlane(tid >> 6);
  const int r = lane & 15, g = lane >> 4;
  int b = blockIdx.x;
  if (b < K2_GEMM) {
    const int i = b >> 3, Mt = (b & 7) * 4 + i / 6, jn = i % 6;
    const int pr = wv % 3, kh = wv / 3, P = jn * 3 + pr;
    const rsrc_t rh = mk(ws + O_HF, ACT), rw = mk(ws + O_W2, WSM), rq = mk(q, ACT), rs1 = mk(ws + O_ST1, S_ * 8);
#pragma unroll
    for (int n = 0; n < 12; ++n) glds16(rh, sm + (n * 6 + wv) * 1024, lane * 16, Mt * 73728 + (n * 6 + wv) * 1024);
    FENCE();
    u32x4 a[9][2];
#pragma unroll
    for (int s = 0; s < 9; ++s)
#pragma unroll
      for (int t = 0; t < 2; ++t) a[s][t] = ld128(rw, lane * 16, (P * 18 + kh * 9 + s) * 2048 + t * 1024);
    u32x4 xq[2];
    u32x2 s1[2];
#pragma unroll
    for (int u = 0; u < 2; ++u) {
      const int row0 = Mt * 64 + (2 * kh + u) * 16;
      xq[u] = ld128(rq, r * 1152 + g * 16, row0 * 1152 + P * 64);
      s1[u] = ld64(rs1, r * 8, row0 * 8);
    }
    FENCE();
    TS(2);
    WAITVM(22);
    BAR();
    TS(3);
    f32x4 acc[2][4] = {};
#pragma unroll
    for (int s = 0; s < 9; ++s) {
#pragma unroll
      for (int t = 0; t < 4; ++t) {
        const u32x4 x = *(const u32x4*)(sm + (t * 18 + kh * 9 + s) * 1024 + lane * 16);
        acc[0][t] = mfma(a[s][0], x, acc[0][t]);
        acc[1][t] = mfma(a[s][1], x, acc[1][t]);
      }
    }
    TS(4);
    BAR();
    {
      char* e = sm + wv * 4096 + lane * 16;
#pragma unroll
      for (int n = 0; n < 2; ++n) {
        *(f32x4*)(e + (n * 2 + 0) * 1024) = kh ? acc[n][0] : acc[n][2];
        *(f32x4*)(e + (n * 2 + 1) * 1024) = kh ? acc[n][1] : acc[n][3];
      }
    }
    BAR();
    {
      const char* e = sm + (kh ? wv - 3 : wv + 3) * 4096 + lane * 16;
      const rsrc_t rx = mk(ws + O_X1, ACT), rs2 = mk(ws + O_ST2, 18 * S_ * 8);
#pragma unroll
      for (int u = 0; u < 2; ++u) {
        const int mt = Mt * 4 + 2 * kh + u;
        const f32x4 v0 = (kh ? acc[0][2 + u] : acc[0][u]) + *(const f32x4*)(e + u * 1024);
        const f32x4 v1 = (kh ? acc[1][2 + u] : acc[1][u]) + *(const f32x4*)(e + (2 + u) * 1024);
        const f32x2 st = __builtin_bit_cast(f32x2, s1[u]);
        u32x4 o;
        float su = 0.f, sq = 0.f;
#pragma unroll
        for (int p = 0; p < 4; ++p) {
          const unsigned d = pk2(blo(xq[u][p]) - st[0], bhi(xq[u][p]) - st[0]);
          const unsigned xn = pk2(blo(d) * st[1], bhi(d) * st[1]);
          const unsigned hs = pk2(v0[p], v1[p]);
          o[p] = pk2(blo(xn) + blo(hs), bhi(xn) + bhi(hs));
          const float f0 = blo(o[p]), f1 = bhi(o[p]);
          su += f0 + f1;
          sq = __builtin_fmaf(f0, f0, __builtin_fmaf(f1, f1, sq));
        }
        st128(o, rx, lane * 16, (mt * 18 + P) * 1024);
        su += shx(su, lane ^ 16);
        sq += shx(sq, lane ^ 16);
        su += shx(su, lane ^ 32);
        sq += shx(sq, lane ^ 32);
        if (g == 0) st64(__builtin_bit_cast(u32x2, f32x2{su, sq}), rs2, r * 8, (P * S_ + mt * 16) * 8);
      }
    }
    TS_END(1, ws);
    return;
  }
  b -= K2_GEMM;
  if (b < K2_CONV) {
    if (wv >= 4) {
      const int PP = b >> 1, P0 = (b & 1) * 9 + (wv - 4) * 5, np = 9 - wv;
      const int sp = PP / 18, ks = PP % 18;
      const rsrc_t rw = mk(w4, WBG), ro = mk(ws + O_W4, WBG);
      unsigned w[5][8];
#pragma unroll
      for (int j = 0; j < 8; ++j) {
        const int T = 2 * PP + (j >> 2), f0 = (T >> 3) * 128 + (T & 7) + (j & 3) * 8;
#pragma unroll
        for (int h = 0; h < 5; ++h)
          w[h][j] = ld32(rw, h < np ? g * 32 * 1152 + r * 4 : 0x80000000u, f0 * 1152 + (P0 + h) * 64);
      }
      FENCE();
      __builtin_amdgcn_s_barrier();
#pragma unroll
      for (int h = 0; h < 5; ++h) {
        if (h < np) {
          u32x4 a0, a1;
#pragma unroll
          for (int p = 0; p < 4; ++p) {
            a0[p] = plo(w[h][2 * p], w[h][2 * p + 1]);
            a1[p] = phi(w[h][2 * p], w[h][2 * p + 1]);
          }
          const int T0 = 2 * (P0 + h), T1 = T0 + 1;
          st128(a0, ro, lane * 16, ((sp * 12 + T0 / 3) * 54 + ks * 3 + T0 % 3) * 1024);
          st128(a1, ro, lane * 16, ((sp * 12 + T1 / 3) * 54 + ks * 3 + T1 % 3) * 1024);
        }
      }
      TS_END(1, ws);
    return;
    }
    const int c1 = wv >> 1, hf = wv & 1, ch = 2 * b + c1;
    char* cb = sm + c1 * CV_CH;
    const rsrc_t rkv = mk(ws + O_KV, ACT), rk = mk(ssk, ACT);
#pragma unroll
    for (int n = 0; n < 2; ++n) {
      const int c = (hf * 2 + n) * 64 + lane;
      glds16(rkv, cb + CV_KV + 512 + (hf * 2 + n) * 1024, (c >> 2) * 128 + (c & 3) * 16, b * 8192 + c1 * 64);
    }
    u32x4 kc[2];
    unsigned kn[2];
#pragma unroll
    for (int n = 0; n < 2; ++n) {
      const int c = (hf * 2 + n) * 64 + lane;
      kc[n] = ld128(rk, ch * 4096 + c * 16, 0);
      kn[n] = ld32(rk, ch * 4096 + c * 16 + 16, 0);
    }
    const unsigned dd = ld32(mk(ssd, D_ * 2), b * 4, 0);
    if (hf == 0) {
      if (lane < 32) *(u32x4*)(cb + CV_KV + lane * 16) = u32x4{0, 0, 0, 0};
      if (lane < 2) *(u32x4*)(cb + CV_E + lane * 16) = u32x4{0, 0, 0, 0};
      if (lane == 0) {
        *(u32x4*)(cb + CV_O) = u32x4{0, 0, 0, 0};
        *(u32x4*)(cb + CV_O + 16) = u32x4{0, 0, 0, kc[0][0] << 16};
      }
    }
#pragma unroll
    for (int n = 0; n < 2; ++n) {
      const int c = (hf * 2 + n) * 64 + lane;
      *(u32x4*)(cb + CV_E + 32 + c * 16) = kc[n];
      const u32x4 o = {__builtin_amdgcn_alignbyte(kc[n][1], kc[n][0], 2), __builtin_amdgcn_alignbyte(kc[n][2], kc[n][1], 2),
                       __builtin_amdgcn_alignbyte(kc[n][3], kc[n][2], 2), __builtin_amdgcn_alignbyte(kn[n], kc[n][3], 2)};
      *(u32x4*)(cb + CV_O + 32 + c * 16) = o;
    }
    TS(2);
    WAITVM(0);
    BAR();
    TS(3);
    f32x4 acc[8] = {};
    const unsigned* ka = (const unsigned*)(cb + ((r & 1) ? CV_E : CV_O)) + ((r + 1) >> 1) + 4 * g + hf * 64;
    const char* kb = cb + CV_KV + 512 + (2 * r + 1 - g - hf * 16) * 16;
#pragma unroll
    for (int u = 0; u < 4; ++u) {
      u32x4 A[8], B[8];
#pragma unroll
      for (int i = 0; i < 8; ++i) {
        const unsigned* p = ka + u * 16 + i * 128;
        A[i] = u32x4{p[0], p[1], p[2], p[3]};
        B[i] = *(const u32x4*)(kb + (i * 32 - u * 4) * 16);
      }
#pragma unroll
      for (int i = 0; i < 8; ++i)
#pragma unroll
        for (int m = 0; m + i < 8; ++m) acc[i + m] = mfma(A[i], B[m], acc[i + m]);
    }
#pragma unroll
    for (int n = 0; n < 8; ++n) *(f32x4*)(sm + CV_Y + wv * 8192 + n * 1024 + r * 64 + g * 16) = acc[n];
    TS(4);
    BAR();
    TS(5);
    const rsrc_t rz = mk(ws + O_Z, ACT);
#pragma unroll
    for (int n = 0; n < 2; ++n) {
      const int id = n * 256 + tid, sb = id >> 3, cc = (id >> 2) & 1, c = sb * 4 + (id & 3);
      const char* y0 = sm + CV_Y + cc * 16384 + c * 32;
      const f32x4 ya = *(const f32x4*)y0 + *(const f32x4*)(y0 + 8192);
      const f32x4 yb = *(const f32x4*)(y0 + 16) + *(const f32x4*)(y0 + 8192 + 16);
      const u32x4 kv = *(const u32x4*)(sm + cc * CV_CH + CV_KV + 512 + c * 16);
      const float dv = cc ? bhi(dd) : blo(dd);
      u32x4 o;
#pragma unroll
      for (int p = 0; p < 4; ++p) {
        const unsigned yy = p < 2 ? pk2(ya[2 * p], ya[2 * p + 1]) : pk2(yb[2 * p - 4], yb[2 * p - 3]);
        const unsigned kd = pk2(bhi(kv[3 - p]) * dv, blo(kv[3 - p]) * dv);
        o[p] = pk2(blo(yy) + blo(kd), bhi(yy) + bhi(kd));
      }
      st128(o, rz, sb * (D_ * 64) + (id & 7) * 16, b * 128);
    }
    TS_END(1, ws);
    return;
  }
  b -= K2_CONV;
  {
    const int f = b * 384 + tid;
    const rsrc_t rc = mk(ws + O_CSP, 18 * F_ * 4);
    float s = 0.f;
#pragma unroll
    for (int j = 0; j < 18; ++j) s += __builtin_bit_cast(float, ld32(rc, f * 4, j * F_ * 4));
    ((float*)(ws + O_CS))[f] = s;
    TS_END(1, ws);
  }
}

constexpr int L3_X = 0, L3_H = 73728, L3_CS = 147456, L3_ST = L3_CS + 3072, L3_FL = L3_ST + 512, L3_PF = L3_FL + 256,
              LDS3 = L3_PF + 256;
#define RING 6

template <int P0, int P1, int P2> DEVI void prio_by_group(int grp) {
  asm volatile(
      "s_setprio %1\n\t"
      "s_cmp_eq_u32 %0, 0\n\t"
      "s_cbranch_scc1 1f\n\t"
      "s_setprio %2\n\t"
      "s_cmp_eq_u32 %0, 1\n\t"
      "s_cbranch_scc1 1f\n\t"
      "s_setprio %3\n\t"
      "1:" ::"s"(grp), "n"(P0), "n"(P1), "n"(P2)
      : "scc");
}

__global__ __launch_bounds__(768, 3) void k3(char* __restrict__ ws) {
  extern __shared__ __attribute__((aligned(16))) char sm[];
  TS_DECL;
  const int tid = threadIdx.x, lane = tid & 63;
  const int wv = __builtin_amdgcn_readfirstlane(tid >> 6);
  const int r = lane & 15, g = lane >> 4;
#define SPX 2
#define PF 0
#define GP 0
  const int xc = blockIdx.x & 7, ix = blockIdx.x >> 3, grp = wv >> 2;
  const int sp = SPX * (xc % (8 / SPX)) + ix % SPX, ml = ix / SPX, Mt = (xc / (8 / SPX)) * (32 / SPX) + ml;
  const rsrc_t rw = mk(ws + O_W3, 2 * WBG), rx = mk(ws + O_X1, ACT);
  const unsigned l16 = lane * 16;
  const unsigned wb = (sp * 12 + wv) * 55296;
#pragma unroll
  for (int n = 0; n < 6; ++n) {
    const int pc = n * 12 + wv;
    glds16(rx, sm + L3_X + pc * 1024, l16, Mt * 73728 + ((pc & 3) * 18 + (pc >> 2)) * 1024);
  }
  if ((PF & 1) && wv >= 4 && wv < 7) {
    const int ln = (wv - 4) * 64 + lane;
#pragma unroll
    for (int n = 0; n < SPX; ++n)
      glds4(rw, sm + L3_PF, ln < 162 ? ln * 128 : 0x80000000u, sp * 663552 + (ml * SPX + n) * 20736);
  }
  if (wv == 10) {
    const rsrc_t rc = mk(ws + O_CS, 20480);
#pragma unroll
    for (int n = 0; n < 3; ++n) glds16(rc, sm + L3_CS + n * 1024, l16, sp * 2304 + n * 1024);
  }
  if (wv == 11) {
    const rsrc_t rs2 = mk(ws + O_ST2, 18 * S_ * 8);
    float su = 0.f, sq = 0.f;
#pragma unroll
    for (int j = 0; j < 18; ++j) {
      const f32x2 v = __builtin_bit_cast(f32x2, ld64(rs2, lane * 8, (j * S_ + Mt * 64) * 8));
      su += v[0];
      sq += v[1];
    }
    const float mean = su * (1.0f / D_);
    const float var = __builtin_fmaxf(sq * (1.0f / D_) - mean * mean, 0.0f);
    const float rs = 1.0f / rbf(__builtin_sqrtf(rbf(rbf(var) + 1e-5f)));
    *(f32x2*)(sm + L3_ST + lane * 8) = f32x2{rs, -mean * rs};
    if (lane < 4) *(unsigned*)(sm + L3_FL + lane * 4) = 0;
  }
  FENCE();
  u32x4 a[RING][3];
#pragma unroll
  for (int i = 0; i < RING - 1; ++i)
#pragma unroll
    for (int t = 0; t < 3; ++t) a[i][t] = ld128(rw, l16, wb + (i * 3 + t) * 1024);
  FENCE();
  asm volatile("s_waitcnt vmcnt(%0)" ::"n"(3 * (RING - 1)) : "memory");
  BAR();
  TS(2);
  if ((PF & 2) && wv >= 8 && wv < 11) {
    const int ln = (wv - 8) * 64 + lane;
#pragma unroll
    for (int n = 0; n < SPX; ++n)
      glds4(rw, sm + L3_PF, ln < 162 ? ln * 128 : 0x80000000u, WBG + sp * 663552 + (ml * SPX + n) * 20736);
  }
  prio_by_group<3, 2, 1>(grp);

  f32x4 acc[3][4];
  u32x4 x[2][4];
  unsigned lb = l16;
#pragma unroll
  for (int t = 0; t < 4; ++t) x[0][t] = *(const u32x4*)(sm + L3_X + t * 1024 + l16);
  sfor<0, 36>([&](auto ic) __attribute__((always_inline)) {
    constexpr int i = decltype(ic)::v;
    constexpr int nx = i + RING - 1;
    if constexpr (nx < 36) {
#pragma unroll
      for (int t = 0; t < 3; ++t)
        a[nx % RING][t] = ld128(rw, l16, wb + (nx < 18 ? nx * 3 + t : (nx - 18) * 3 + t) * 1024 + (nx < 18 ? 0 : WBG));
    }
    if constexpr (i == 18) {
      TS(3);
      __builtin_amdgcn_s_setprio(GP);
      f32x2 st[4];
#pragma unroll
      for (int m = 0; m < 4; ++m) st[m] = *(const f32x2*)(sm + L3_ST + (m * 16 + r) * 8);
#pragma unroll
      for (int t = 0; t < 3; ++t) {
        const int tl = wv * 3 + t;
        const f32x4 c = *(const f32x4*)(sm + L3_CS + tl * 64 + g * 16);
        char* hp = sm + L3_H + (tl >> 1) * 4096 + (tl & 1) * 8 + l16;
#pragma unroll
        for (int m = 0; m < 4; ++m) {
          float h[4];
#pragma unroll
          for (int p = 0; p < 4; ++p) h[p] = gelu_t(__builtin_fmaf(acc[t][m][p], st[m][0], st[m][1] * c[p]));
          *(u32x2*)(hp + m * 1024) = u32x2{pk2(h[0], h[1]), pk2(h[2], h[3])};
        }
      }
      asm volatile("s_waitcnt lgkmcnt(0)" ::: "memory");
      sm[L3_FL + wv] = 1;
      if (GP) __builtin_amdgcn_s_setprio(0);
      TS(4);
      wait_flag<0>(L3_FL);
#pragma unroll
      for (int t = 0; t < 4; ++t) x[0][t] = *(const u32x4*)(sm + (24 + t) * 1024 + lb);
    }
    if constexpr (i == 23) wait_flag<4>(L3_FL);
    if constexpr (i == 29) {
      wait_flag<8>(L3_FL);
      prio_by_group<3, 2, 0>(grp);
    }
    if constexpr (i == 11 || i == 23) asm volatile("v_add_u32 %0, 0xc000, %0" : "+v"(lb));
    if constexpr (i != 17 && i != 35) {
#pragma unroll
      for (int t = 0; t < 4; ++t) x[(i + 1) & 1][t] = *(const u32x4*)(sm + (((i + 1) % 12) * 4 + t) * 1024 + lb);
    }
    FENCE();
#pragma unroll
    for (int m = 0; m < 4; ++m)
#pragma unroll
      for (int t = 0; t < 3; ++t)
        acc[t][m] = mfma(a[i % RING][t], x[i & 1][m], (i == 0 || i == 18) ? f32x4{0, 0, 0, 0} : acc[t][m]);
    FENCE();
  });
  TS(5);
  const rsrc_t rp = mk(ws + O_PART, 8 * ACT);
#pragma unroll
  for (int t = 0; t < 3; ++t)
#pragma unroll
    for (int h = 0; h < 2; ++h) {
      const f32x4 v0 = acc[t][2 * h], v1 = acc[t][2 * h + 1];
      const u32x4 o = {pk2(v0[0], v0[1]), pk2(v0[2], v0[3]), pk2(v1[0], v1[1]), pk2(v1[2], v1[3])};
      st128a<ST_AUX3>(o, rp, l16, sp * ACT + Mt * 73728 + ((wv * 3 + t) * 2 + h) * 1024);
    }
  TS_END(2, ws);
}

__global__ __launch_bounds__(256) void k4(const void* __restrict__ q, const char* __restrict__ ws, void* __restrict__ out) {
  __shared__ __attribute__((aligned(16))) char sm[2 * 8704 + 4096];
  TS_DECL;
  const int tid = threadIdx.x, lane = tid & 63;
  const int wv = __builtin_amdgcn_readfirstlane(tid >> 6);
  const int r = lane & 15, g = lane >> 4;
  const int G = blockIdx.x % 9, sb = blockIdx.x / 9, Mt = sb >> 1, mp = sb & 1;
  const int pl = wv & 1, sh = wv >> 1, P = 2 * G + pl;
  const rsrc_t rp = mk(ws + O_PART, 8 * ACT), rx = mk(ws + O_X1, ACT), rz = mk(ws + O_Z, ACT), rq = mk(q, ACT),
               rs1 = mk(ws + O_ST1, S_ * 8), ro = mk(out, ACT);
  u32x4 pp[4][2];
#pragma unroll
  for (int s = 0; s < 4; ++s)
#pragma unroll
    for (int t = 0; t < 2; ++t)
      pp[s][t] = ld128(rp, lane * 16, (sh * 4 + s) * ACT + Mt * 73728 + ((2 * P + t) * 2 + mp) * 1024);
  const u32x4 x1 = ld128(rx, lane * 16, ((sb * 2 + sh) * 18 + P) * 1024);
  const u32x4 zz = ld128(rz, tid * 16, sb * (D_ * 64) + G * 4096);
  const int row = tid >> 3, c8 = tid & 7;
  const unsigned ooff = row * 1152 + c8 * 16, osf = sb * 32 * 1152 + G * 128;
  const u32x4 xq = ld128(rq, ooff, osf);
  const f32x2 st = __builtin_bit_cast(f32x2, ld64(rs1, row * 8, sb * 256));
  FENCE();
  *(u32x4*)(sm + 17408 + (tid ^ ((tid >> 5) & 3)) * 16) = zz;
#pragma unroll
  for (int i = 0; i < 2; ++i) {
    float v[8];
#pragma unroll
    for (int p = 0; p < 4; ++p)
#pragma unroll
      for (int t = 0; t < 2; ++t) {
        float a = 0.f;
#pragma unroll
        for (int s = 0; s < 4; ++s) a += (p & 1) ? bhi(pp[s][t][2 * i + (p >> 1)]) : blo(pp[s][t][2 * i + (p >> 1)]);
        const float xv = t ? bhi(x1[p]) : blo(x1[p]);
        v[2 * p + t] = a + (i == sh ? xv : 0.f);
      }
    char* e = sm + sh * 8704 + (i * 16 + r) * 272 + pl * 128 + g * 32;
    *(f32x4*)e = f32x4{v[0], v[1], v[2], v[3]};
    *(f32x4*)(e + 16) = f32x4{v[4], v[5], v[6], v[7]};
  }
  TS(2);
  BAR();
  TS(3);
  const char* e = sm + row * 272 + c8 * 32;
  const f32x4 b0 = *(const f32x4*)e + *(const f32x4*)(e + 8704);
  const f32x4 b1 = *(const f32x4*)(e + 16) + *(const f32x4*)(e + 8704 + 16);
  const char* zp = sm + 17408 + c8 * 512 + (((row >> 3) ^ (c8 & 3)) * 16) + (row & 7) * 2;
  float o[8];
#pragma unroll
  for (int j = 0; j < 8; ++j) {
    const float bo = rbf(j < 4 ? b0[j] : b1[j - 4]);
    const float gt = rbf(bo * __builtin_amdgcn_rcpf(1.0f + __builtin_amdgcn_exp2f(bo * -1.442695041f)));
    const float z = blo(*(const unsigned short*)(zp + j * 64));
    const float xv = (j & 1) ? bhi(xq[j >> 1]) : blo(xq[j >> 1]);
    const float xn = rbf(rbf(xv - st[0]) * st[1]);
    o[j] = xv + rbf(rbf(xn * z) * gt);
  }
  st128a<0>(u32x4{pk2(o[0], o[1]), pk2(o[2], o[3]), pk2(o[4], o[5]), pk2(o[6], o[7])}, ro, ooff, osf);
  TS_END(3, ws);
}

#if defined(__HIP_DEVICE_COMPILE__) && !defined(PKOK)
#pragma clang attribute pop
#endif

extern "C" void kernel_launch(void* const* d_in, const int* in_sizes, int n_in, void* d_out, int out_size, void* d_ws,
                              size_t ws_size, hipStream_t stream) {
  (void)in_sizes; (void)n_in; (void)out_size; (void)ws_size;
  char* ws = (char*)d_ws;
  (void)hipFuncSetAttribute((const void*)k1, hipFuncAttributeMaxDynamicSharedMemorySize, LDS1);
  (void)hipFuncSetAttribute((const void*)k2, hipFuncAttributeMaxDynamicSharedMemorySize, LDS12);
  (void)hipFuncSetAttribute((const void*)k3, hipFuncAttributeMaxDynamicSharedMemorySize, LDS3);
  k1<<<256, 896, LDS1, stream>>>(d_in[0], d_in[1], d_in[2], d_in[3], d_in[7], d_in[8], d_in[9], ws);
  k2<<<K2_GEMM + K2_CONV + K2_CS, 384, LDS12, stream>>>(d_in[0], d_in[4], d_in[5], d_in[10], ws);
  k3<<<256, 768, LDS3, stream>>>(ws);
  k4<<<576, 256, 0, stream>>>(d_in[0], ws, d_out);
}
```

```cpp
#include <hip/hip_runtime.h>
#include <hip/hip_bf16.h>
#include <stdint.h>

typedef __attribute__((ext_vector_type(8))) __bf16 bf16x8;
typedef __attribute__((ext_vector_type(2))) __bf16 bf16x2;
typedef __attribute__((ext_vector_type(4))) float f32x4;
typedef __attribute__((ext_vector_type(2))) float f32x2;
typedef __attribute__((ext_vector_type(4))) unsigned u32x4;
typedef __attribute__((ext_vector_type(2))) unsigned u32x2;
typedef __amdgpu_buffer_rsrc_t rsrc_t;
typedef __attribute__((address_space(3))) void* lds_ptr;

#define DEVI __device__ __forceinline__
#if defined(__HIP_DEVICE_COMPILE__) && !defined(PKOK)
#pragma clang attribute push(__attribute__((target("no-packed-fp32-ops"))), apply_to = function)
#endif

constexpr int S_ = 2048, D_ = 576, F_ = 4608;
constexpr int ACT = S_ * D_ * 2;
constexpr int WSM = D_ * D_ * 2;
constexpr int WBG = D_ * F_ * 2;
constexpr size_t O_HF = 0;
constexpr size_t O_KV = O_HF + ACT;
constexpr size_t O_X1 = O_KV + ACT;
constexpr size_t O_Z = O_X1 + ACT;
constexpr size_t O_W2 = O_Z + ACT;
constexpr size_t O_W3 = O_W2 + WSM;
constexpr size_t O_W4 = O_W3 + WBG;
constexpr size_t O_CSP = O_W4 + WBG;
constexpr size_t O_CS = O_CSP + 18 * F_ * 4;
constexpr size_t O_ST1 = O_CS + 20480;
constexpr size_t O_ST2 = O_ST1 + S_ * 8;
#define FSP 256
constexpr size_t O_FLG = O_ST2 + 18 * S_ * 8;
constexpr size_t O_PART = O_FLG + 384 * FSP;

#define ST_AUX 16

DEVI rsrc_t mk(const void* p, int bytes) {
  return __builtin_amdgcn_make_buffer_rsrc((void*)p, (short)0, bytes, 0x00020000);
}
DEVI unsigned ld32(rsrc_t r, unsigned v, unsigned s) { return __builtin_amdgcn_raw_buffer_load_b32(r, v, s, 0); }
DEVI u32x2 ld64(rsrc_t r, unsigned v, unsigned s) { return __builtin_amdgcn_raw_buffer_load_b64(r, v, s, 0); }
DEVI u32x4 ld128(rsrc_t r, unsigned v, unsigned s) { return __builtin_amdgcn_raw_buffer_load_b128(r, v, s, 0); }
template <int AUX> DEVI void st128a(u32x4 d, rsrc_t r, unsigned v, unsigned s) {
  __builtin_amdgcn_raw_buffer_store_b128(d, r, v + s, 0, AUX);
}
DEVI void st128(u32x4 d, rsrc_t r, unsigned v, unsigned s) { st128a<ST_AUX>(d, r, v, s); }
DEVI void st64(u32x2 d, rsrc_t r, unsigned v, unsigned s) { __builtin_amdgcn_raw_buffer_store_b64(d, r, v, s, ST_AUX); }
DEVI void glds16(rsrc_t r, char* lds, unsigned v, unsigned s) {
  __builtin_amdgcn_raw_ptr_buffer_load_lds(r, (lds_ptr)lds, 16, v, s, 0, 0);
}
DEVI void glds4(rsrc_t r, char* lds, unsigned v, unsigned s) {
  __builtin_amdgcn_raw_ptr_buffer_load_lds(r, (lds_ptr)lds, 4, v, s, 0, 0);
}
DEVI unsigned pk2(float a, float b) {
  bf16x2 v = {(__bf16)a, (__bf16)b};
  return __builtin_bit_cast(unsigned, v);
}
DEVI float blo(unsigned u) { return __builtin_bit_cast(float, u << 16); }
DEVI float bhi(unsigned u) { return __builtin_bit_cast(float, u & 0xffff0000u); }
DEVI float rbf(float a) { return (float)(__bf16)a; }
DEVI f32x4 mfma(u32x4 a, u32x4 b, f32x4 c) {
  return __builtin_amdgcn_mfma_f32_16x16x32_bf16(__builtin_bit_cast(bf16x8, a), __builtin_bit_cast(bf16x8, b), c, 0, 0, 0);
}
DEVI unsigned plo(unsigned a, unsigned b) { return __builtin_amdgcn_perm(b, a, 0x05040100u); }
DEVI unsigned phi(unsigned a, unsigned b) { return __builtin_amdgcn_perm(b, a, 0x07060302u); }
DEVI float shx(float v, int src) {
  return __builtin_bit_cast(float, __builtin_amdgcn_ds_bpermute(src * 4, __builtin_bit_cast(int, v)));
}
DEVI float gelu_t(float x) {
  const float c0 = -2.302208198f, c1 = -0.1029432397f;
  float e = __builtin_amdgcn_exp2f(x * __builtin_fmaf(x * x, c1, c0));
  return x * __builtin_amdgcn_rcpf(1.0f + e);
}
#define WAITVM(n) asm volatile("s_waitcnt vmcnt(" #n ")" ::: "memory")
#define BAR()                                          \
  do {                                                 \
    asm volatile("s_waitcnt lgkmcnt(0)" ::: "memory"); \
    __builtin_amdgcn_s_barrier();                      \
    asm volatile("" ::: "memory");                     \
  } while (0)
#define FENCE() __builtin_amdgcn_sched_barrier(0)

#define TS_DECL
#define TS(i)
#define TS_END(kn, wsp)

template <int V> struct IC { static constexpr int v = V; };
template <int I, int N, class Fn> DEVI void sfor(Fn&& f) {
  if constexpr (I < N) {
    f(IC<I>{});
    sfor<I + 1, N>(f);
  }
}

constexpr unsigned ONES = 0x3f803f80u;
constexpr int LDS12 = 73728;

template <int OFF> DEVI void wait_flag(unsigned zero) {
  unsigned tv, ts;
  asm volatile(
      "1:\n\t"
      "ds_read_b32 %0, %2 offset:%3\n\t"
      "s_waitcnt lgkmcnt(0)\n\t"
      "v_readfirstlane_b32 %1, %0\n\t"
      "s_cmp_eq_u32 %1, 0x01010101\n\t"
      "s_cbranch_scc1 2f\n\t"
      "s_sleep 2\n\t"
      "s_branch 1b\n\t"
      "2:"
      : "=&v"(tv), "=&s"(ts)
      : "v"(zero), "n"(OFF)
      : "memory", "scc");
}
constexpr int T1R = 74, C1X = 75 * 384, C1 = C1X + 192 * 128, L1_FL = 3 * C1, LDS1 = L1_FL + 64;
constexpr int U_W3 = 648, U_KV = 576, U_W2 = 162;
typedef __attribute__((ext_vector_type(4))) short s16x4;
DEVI u32x2 ldtr(const char* p) {
  return __builtin_bit_cast(u32x2, __builtin_amdgcn_ds_read_tr16_b64_v4i16((__attribute__((address_space(3))) s16x4*)p));
}
DEVI float bc15(float v) {
  return __builtin_bit_cast(float, __builtin_amdgcn_update_dpp(0, __builtin_bit_cast(int, v), 0x15f, 0xf, 0xf, false));
}
#define KEEP(x) asm volatile("" : "+v"(x))
#define PASSES(p0) int pass = p0; asm volatile("" : "+s"(pass)); _Pragma("unroll 1") for (; pass < 2; ++pass)

__global__ __launch_bounds__(896, 4) void k1(const void* __restrict__ q, const void* __restrict__ kin,
                                             const void* __restrict__ vin, const void* __restrict__ shk,
                                             const void* __restrict__ w1, const void* __restrict__ w2,
                                             const void* __restrict__ w3, char* __restrict__ ws) {
  extern __shared__ __attribute__((aligned(16))) char sm[];
  TS_DECL;
  const int tid = threadIdx.x, lane = tid & 63;
  const int wv = __builtin_amdgcn_readfirstlane(tid >> 6);
  const int r = lane & 15, g = lane >> 4;
  const int b = blockIdx.x, tile = (b & 7) * 32 + (b >> 3), Mt = tile / 9, Nt = tile - Mt * 9;
#define ROT 1
#define LB 0
#define SE 3
#define SF 1
#define GD 1
  if (wv >= 4 && wv < 8) {
    if (tile >= 252) BAR();
    if (tile >= 252) return;
    const int w = wv - 4;
    const rsrc_t rq = mk(q, ACT), rw = mk(w1, WSM);
    unsigned xo[7];
#pragma unroll
    for (int j = 0; j < 7; ++j) {
      const int L = (j * 4 + w) * 64 + lane, row = L / 24;
      xo[j] = row * 1152 + (((L - row * 24) ^ ((row >> 1) & 7)) * 16);
    }
    const unsigned wo = (w * 8 + (lane >> 3)) * 1152 + (((lane & 7) ^ ((lane >> 2) & 4)) * 16);
    int cc = ROT ? (Mt + Nt) % 3 : 0;
#pragma unroll
    for (int j = 0; j < 7; ++j) KEEP(xo[j]);
    if (!LB) __builtin_amdgcn_s_barrier();
#pragma unroll 1
    for (int c = 0; c < 3; ++c, cc = cc == 2 ? 0 : cc + 1) {
      char* cb = sm + c * C1 + w * 1024;
#pragma unroll
      for (int j = 0; j < 7; ++j) {
        if (j < 6 || w < 3 || lane < 48) glds16(rq, cb + j * 4096, xo[j], Mt * (T1R * 1152) + cc * 384);
        if (j < 6) glds16(rw, cb + C1X + j * 4096, wo, (cc * 192 + j * 32) * 1152 + Nt * 128);
      }
      if (LB && !c) __builtin_amdgcn_s_barrier();
      if (c) {
        WAITVM(13);
        sm[L1_FL + (c - 1) * 4 + w] = 1;
      }
    }
    TS(2);
    WAITVM(0);
    sm[L1_FL + 8 + w] = 1;
    TS_END(0, ws);
    return;
  }
  const u32x4 ones = {ONES, ONES, ONES, ONES};
  if (wv < 3 && lane < 24) *(u32x4*)(sm + wv * C1 + T1R * 384 + lane * 16) = ones;
  if (wv == 3 && lane < 4) *(unsigned*)(sm + L1_FL + lane * 4) = 0;
  if (!SF || wv < 4) BAR();
  if (wv < 8 && tile >= 252) return;
  if (wv < 4) {
    const int pr = wv & 1, mh = wv >> 1, P = Nt * 2 + pr;
    const unsigned wa = C1X + (8 * g + (r >> 2)) * 128 + (((pr * 4 + (r & 3)) ^ ((r >> 3) << 2)) * 16);
    unsigned xa[3][2];
#pragma unroll
    for (int m = 0; m < 3; ++m) {
      const int mt = mh * 3 + m > 4 ? 4 : mh * 3 + m, row = r == 15 ? T1R : mt * 15 + r, sw = (row >> 1) & 7;
      xa[m][0] = row * 384 + ((g ^ sw) * 16);
      xa[m][1] = row * 384 + (((4 + g) ^ sw) * 16);
    }
    const rsrc_t rh = mk(ws + O_HF, ACT), rs1 = mk(ws + O_ST1, S_ * 8);
    const int grp = (P * 4 + g) / 18;
    const int sh = grp == 0 ? 1 : grp == 1 ? -1 : grp == 2 ? 2 : -2;
    PASSES(GD) {
      f32x4 acc[3][2] = {}, gm[3] = {};
#pragma unroll 1
      for (int c = pass ? 0 : 2; c < 3; ++c) {
        if (pass) wait_flag<0>(L1_FL + c * 4);
        if (c == 0) TS(2);
        if (c == 2) TS(3);
        const char* cb = sm + c * C1;
        u32x4 a[2][2], x[2][3];
#pragma unroll
        for (int s = 0; s < 7; ++s) {
          u32x2 hf[4];
          const int u = (s - 1) & 1;
#pragma unroll
          for (int j = 0; j < 9; ++j) {
            if (s > 0) {
              if (j < 3) gm[j] = mfma(x[u][j], x[u][j], gm[j]);
              else acc[j % 3][j / 3 - 1] = mfma(a[u][j / 3 - 1], x[u][j % 3], acc[j % 3][j / 3 - 1]);
            }
            FENCE();
            if (s < 6) {
              if (j < 3) x[s & 1][j] = *(const u32x4*)(cb + xa[j][s & 1] + (s >> 1) * 128);
              else if (j < 7) hf[j - 3] = ldtr(cb + wa + s * 4096 + ((j - 3) & 1) * 512 + ((j - 3) >> 1) * 8);
            }
            FENCE();
          }
          if (s < 6) {
            a[s & 1][0] = u32x4{hf[0][0], hf[0][1], hf[1][0], hf[1][1]};
            a[s & 1][1] = u32x4{hf[2][0], hf[2][1], hf[3][0], hf[3][1]};
          }
        }
      }
      TS(4);
#pragma unroll 1
      for (int m = 0; m < (pass ? 3 - mh : 1); ++m) {
        const float su = shx(gm[0][3], 48 + r);
        const float d0 = (r & 1) ? gm[0][1] : gm[0][0], d1 = (r & 1) ? gm[0][3] : gm[0][2];
        const float sq = shx((r & 2) ? d1 : d0, r + 16 * (r >> 2));
        const float mean = su * (1.0f / D_);
        const float var = __builtin_fmaxf(sq * (1.0f / D_) - mean * mean, 0.0f);
        const float r1 = __builtin_amdgcn_rcpf(rbf(__builtin_amdgcn_sqrtf(rbf(rbf(var) + 1e-5f))));
        const float sc = r1 * __builtin_amdgcn_rsqf(var * r1 * r1 + 1e-5f);
        const float nm = -mean * sc;
        float h[8];
#pragma unroll
        for (int p = 0; p < 8; ++p) {
          const float v = acc[0][p >> 2][p & 3];
          h[p] = gelu_t(__builtin_fmaf(v, sc, nm * bc15(v)));
        }
        u32x4 o = {pk2(h[0], h[1]), pk2(h[2], h[3]), pk2(h[4], h[5]), pk2(h[6], h[7])};
        const int lr = (mh * 3 + m) * 15 + r, row = Mt * T1R + lr;
        const bool ok = r < 15 && lr < T1R && row < S_;
        const int dr = row + sh, dw = dr & (S_ - 1);
        if (dr != dw) o = u32x4{0, 0, 0, 0};
        unsigned ho = ok ? (dw >> 4) * 18432 + (g * 16 + (dw & 15)) * 16 + P * 1024 : 0x80000000u;
        u32x2 sv = __builtin_bit_cast(u32x2, f32x2{rbf(mean), r1});
        KEEP(o);
        KEEP(ho);
        KEEP(sv);
        if (pass) {
          st128(o, rh, ho, 0);
          if (P == 0) st64(sv, rs1, ok && g == 0 ? row * 8 : 0x80000000u, 0);
        }
#pragma unroll
        for (int n = 0; n < 2; ++n) {
          gm[n] = gm[n + 1];
          acc[n][0] = acc[n + 1][0];
          acc[n][1] = acc[n + 1][1];
        }
      }
      if (!pass) TS(5);
    }
    TS_END(0, ws);
    return;
  }
  int un = (wv - 8) * 256 + b;
  if (un < U_W3) {
    const int G = un % 36, ks = un / 36;
    const rsrc_t rw = mk(w3, WBG), ro = mk(ws + O_W3, WBG), rc = mk(ws + O_CSP, 18 * F_ * 4);
    PASSES(SE & 1) {
      u32x4 w[8] = {};
      if (pass) {
#pragma unroll
        for (int j = 0; j < 8; ++j) w[j] = ld128(rw, g * 8 * 9216 + r * 16, (ks * 32 + j) * 9216 + G * 256);
        FENCE();
        if (SF) __builtin_amdgcn_s_barrier();
        FENCE();
      }
      f32x4 mine = {};
#pragma unroll
      for (int t = 0; t < 8; ++t) {
        u32x4 a;
#pragma unroll
        for (int p = 0; p < 4; ++p)
          a[p] = (t & 1) ? phi(w[2 * p][t >> 1], w[2 * p + 1][t >> 1]) : plo(w[2 * p][t >> 1], w[2 * p + 1][t >> 1]);
        const int T = G * 8 + t;
        unsigned o = lane * 16 + ((T / 3) * 54 + ks * 3 + T % 3) * 1024;
        KEEP(a);
        KEEP(o);
        if (pass) st128(a, ro, o, 0);
        const f32x4 c = mfma(a, ones, f32x4{0, 0, 0, 0});
        if (r == t) mine = c;
      }
      KEEP(mine);
      if (pass) st128(__builtin_bit_cast(u32x4, mine), rc, r < 8 ? r * 64 + g * 16 : 0x80000000u, ks * F_ * 4 + G * 512);
      else TS(2);
    }
    TS_END(0, ws);
    return;
  }
  un -= U_W3;
  if (un < U_KV) {
    const int G = un % 9, sb = un / 9;
    const rsrc_t rk = mk(kin, ACT), rv = mk(vin, ACT), rsk = mk(shk, D_ * 8), ro = mk(ws + O_KV, ACT);
    const int row0 = sb * 32 + g * 8;
    const unsigned off = row0 * 1152 + G * 128 + r * 8;
    const int src = ((lane & 3) * 16 + (lane >> 2)) * 4;
    PASSES(SE >> 1) {
      u32x2 kk[11] = {}, vv[8] = {};
      u32x4 s0 = {}, s1 = {};
      if (pass) {
        s0 = ld128(rsk, (G * 64 + r * 4) * 8, 0);
        s1 = ld128(rsk, (G * 64 + r * 4) * 8 + 16, 0);
#pragma unroll
        for (int j = 0; j < 3; ++j) kk[j] = ld64(rk, row0 == 0 ? 0x80000000u : off - (3 - j) * 1152, 0);
#pragma unroll
        for (int j = 0; j < 8; ++j) {
          kk[3 + j] = ld64(rk, off, j * 1152);
          vv[j] = ld64(rv, off, j * 1152);
        }
        FENCE();
        if (SF) __builtin_amdgcn_s_barrier();
        FENCE();
      }
#pragma unroll 1
      for (int th = pass ? 0 : 1; th < 2; ++th) {
#pragma unroll
        for (int t = 0; t < 2; ++t) {
          const float c0 = blo(s0[2 * t]), c1 = bhi(s0[2 * t]), c2 = blo(s0[2 * t + 1]), c3 = bhi(s0[2 * t + 1]);
          float kf[11], o[8];
#pragma unroll
          for (int j = 0; j < 11; ++j) kf[j] = t ? bhi(kk[j][0]) : blo(kk[j][0]);
#pragma unroll
          for (int j = 0; j < 8; ++j) {
            const float ks = __builtin_fmaf(kf[j + 3], c0, __builtin_fmaf(kf[j + 2], c1, __builtin_fmaf(kf[j + 1], c2, kf[j] * c3)));
            o[j] = ks * (t ? bhi(vv[j][0]) : blo(vv[j][0]));
          }
          u32x4 w;
#pragma unroll
          for (int p = 0; p < 4; ++p) w[p] = __builtin_amdgcn_ds_bpermute(src, pk2(o[7 - 2 * p], o[6 - 2 * p]));
          KEEP(w);
          if (pass) st128(w, ro, (lane >> 2) * 16384 + (lane & 3) * 16, (G * 32 + th) * 8192 + sb * 128 + t * 64);
        }
        s0 = s1;
#pragma unroll
        for (int j = 0; j < 11; ++j) {
          kk[j][0] = kk[j][1];
          if (j < 8) vv[j][0] = vv[j][1];
        }
      }
      if (!pass) TS(2);
    }
    TS_END(0, ws);
    return;
  }
  un -= U_KV;
  if (un < U_W2) {
    const int G = un % 9, ks = un / 9;
    const rsrc_t rw = mk(w2, WSM), ro = mk(ws + O_W2, WSM);
    unsigned w[2][8];
#pragma unroll
    for (int j = 0; j < 8; ++j)
#pragma unroll
      for (int h = 0; h < 2; ++h) w[h][j] = ld32(rw, g * 9216 + r * 4, (ks * 32 + j) * 1152 + G * 128 + h * 64);
        FENCE();
        if (SF) __builtin_amdgcn_s_barrier();
        FENCE();
#pragma unroll
    for (int h = 0; h < 2; ++h) {
      u32x4 a0, a1;
#pragma unroll
      for (int p = 0; p < 4; ++p) {
        a0[p] = plo(w[h][2 * p], w[h][2 * p + 1]);
        a1[p] = phi(w[h][2 * p], w[h][2 * p + 1]);
      }
      st128(a0, ro, lane * 16, ((G * 2 + h) * 18 + ks) * 2048);
      st128(a1, ro, lane * 16, ((G * 2 + h) * 18 + ks) * 2048 + 1024);
    }
  }
  TS_END(0, ws);
}

constexpr int K2_GEMM = 192, K2_CONV = 288, K2_CS = 12;
#define EO 4160
constexpr int CV_KV = 0, CV_E = 4608, CV_O = CV_E + EO, CV_CH = CV_O + 4224, CV_Y = 2 * CV_CH;

__global__ __launch_bounds__(384, 4) void k2(const void* __restrict__ q, const void* __restrict__ ssk,
                                             const void* __restrict__ ssd, const void* __restrict__ w4,
                                             char* __restrict__ ws) {
  extern __shared__ __attribute__((aligned(16))) char sm[];
  TS_DECL;
  const int tid = threadIdx.x, lane = tid & 63;
  const int wv = __builtin_amdgcn_readfirstlane(tid >> 6);
  const int r = lane & 15, g = lane >> 4;
#define R2 1
#define GD0 2
#define GDP 6
#define CDP 4
#define CO 288
#define W4L 1
  int b = blockIdx.x;
  if (CO && b < K2_GEMM + CO) b = b < CO ? b + K2_GEMM : b - CO;
  if (b < K2_GEMM) {
    const int i = b >> 3, Mt = (b & 7) * 4 + i / 6, jn = i % 6;
    const int pr = wv % 3, kh = wv / 3, P = jn * 3 + pr;
    const rsrc_t rh = mk(ws + O_HF, ACT), rw = mk(ws + O_W2, WSM), rq = mk(q, ACT), rs1 = mk(ws + O_ST1, S_ * 8);
    u32x4 a[9][2], xq[2];
    u32x2 s1[2];
#define K2_H(n0, n1) _Pragma("unroll") for (int n = n0; n < n1; ++n) { const int m = (n * 6 + wv) * 1024; \
      glds16(rh, sm + m, lane * 16, Mt * 73728 + m); } FENCE()
#define K2_W(n0, n1) _Pragma("unroll") for (int n = n0; n < n1; ++n) \
      a[n >> 1][n & 1] = ld128(rw, lane * 16, (P * 18 + kh * 9 + (n >> 1)) * 2048 + (n & 1) * 1024); FENCE()
#define K2_Q() _Pragma("unroll") for (int u = 0; u < 2; ++u) { const int row0 = Mt * 64 + (2 * kh + u) * 16; \
      xq[u] = ld128(rq, r * 1152 + g * 16, row0 * 1152 + P * 64); s1[u] = ld64(rs1, r * 8, row0 * 8); } FENCE()
#define K2_N() _Pragma("unroll") for (int u = 0; u < 2; ++u) { const f32x2 st = __builtin_bit_cast(f32x2, s1[u]); \
      _Pragma("unroll") for (int p = 0; p < 4; ++p) { const unsigned d = pk2(blo(xq[u][p]) - st[0], bhi(xq[u][p]) - st[0]); \
        xq[u][p] = pk2(blo(d) * st[1], bhi(d) * st[1]); } KEEP(xq[u]); } FENCE()
    K2_Q(); K2_H(0, 12); K2_W(0, 18);
    TS(2);
    K2_N();
    WAITVM(18);
    BAR();
    TS(3);
    f32x4 acc[2][4] = {};
    {
      const char* xb[2] = {sm + kh * (9216 + 36864) + lane * 16, sm + kh * (9216 - 36864) + 36864 + lane * 16};
      u32x4 x[36];
#pragma unroll
      for (int j = -1; j < 36; ++j) {
        if (j >= 0) {
          acc[0][j & 3] = mfma(a[j >> 2][0], x[j], acc[0][j & 3]);
          acc[1][j & 3] = mfma(a[j >> 2][1], x[j], acc[1][j & 3]);
        }
        FENCE();
#define K2_D(j) ((j) < 0 ? 0 : (j) + ((j) < 4 ? GD0 : (j) < 8 ? 4 : GDP))
#pragma unroll
        for (int n = K2_D(j); n < K2_D(j + 1); ++n)
          if (n < 36) x[n] = *(const u32x4*)(xb[(n >> 1) & 1] + (n >> 2) * 1024 + (n & 1) * 18432);
        FENCE();
      }
    }
    TS(4);
    if (!R2) { K2_N(); }
    BAR();
    {
      char* e = sm + wv * 4096 + lane * 16;
#pragma unroll
      for (int n = 0; n < 2; ++n) {
        *(f32x4*)(e + (n * 2 + 0) * 1024) = acc[n][2];
        *(f32x4*)(e + (n * 2 + 1) * 1024) = acc[n][3];
      }
    }
    BAR();
    TS(5);
    {
      const char* e = sm + (kh ? wv - 3 : wv + 3) * 4096 + lane * 16;
      const rsrc_t rx = mk(ws + O_X1, ACT), rs2 = mk(ws + O_ST2, 18 * S_ * 8);
      unsigned tt[2];
#pragma unroll
      for (int u = 0; u < 2; ++u) {
        const int mt = Mt * 4 + 2 * kh + u;
        const f32x4 v0 = acc[0][u] + *(const f32x4*)(e + u * 1024);
        const f32x4 v1 = acc[1][u] + *(const f32x4*)(e + (2 + u) * 1024);
        u32x4 o;
        float su = 0.f, sq = 0.f;
#pragma unroll
        for (int p = 0; p < 4; ++p) {
          const unsigned xn = xq[u][p];
          const unsigned hs = pk2(v0[p], v1[p]);
          o[p] = pk2(blo(xn) + blo(hs), bhi(xn) + bhi(hs));
          const float f0 = blo(o[p]), f1 = bhi(o[p]);
          su += f0 + f1;
          sq = __builtin_fmaf(f0, f0, __builtin_fmaf(f1, f1, sq));
        }
        st128(o, rx, lane * 16, (mt * 18 + P) * 1024);
        const auto w = __builtin_amdgcn_permlane16_swap(__builtin_bit_cast(unsigned, su), __builtin_bit_cast(unsigned, sq), false, false);
        const unsigned w0 = w[0], w1 = w[1];
        tt[u] = __builtin_bit_cast(unsigned, __builtin_bit_cast(float, w0) + __builtin_bit_cast(float, w1));
      }
      const auto w = __builtin_amdgcn_permlane32_swap(tt[0], tt[1], false, false);
      const unsigned w0 = w[0], w1 = w[1];
      const float t = __builtin_bit_cast(float, w0) + __builtin_bit_cast(float, w1);
      __builtin_amdgcn_raw_buffer_store_b32(__builtin_bit_cast(unsigned, t), rs2, (g >> 1) * 128 + r * 8 + (g & 1) * 4,
                                            (P * S_ + (Mt * 4 + 2 * kh) * 16) * 8, ST_AUX);
    }
    TS_END(1, ws);
    return;
  }
  b -= K2_GEMM;
  if (b < K2_CONV) {
    if (wv >= 4) {
      const int PP = b >> 1, P0 = (b & 1) * 9 + (wv - 4) * 5, np = 9 - wv;
      const int sp = PP / 18, ks = PP % 18;
      const rsrc_t rw = mk(w4, WBG), ro = mk(ws + O_W4, WBG);
      unsigned w[5][8];
      if (W4L) __builtin_amdgcn_s_barrier();
      FENCE();
#pragma unroll
      for (int j = 0; j < 8; ++j) {
        const int T = 2 * PP + (j >> 2), f0 = (T >> 3) * 128 + (T & 7) + (j & 3) * 8;
#pragma unroll
        for (int h = 0; h < 5; ++h)
          w[h][j] = ld32(rw, h < np ? g * 32 * 1152 + r * 4 : 0x80000000u, f0 * 1152 + (P0 + h) * 64);
      }
      FENCE();
      if (W4L != 2) __builtin_amdgcn_s_barrier();
#pragma unroll
      for (int h = 0; h < 5; ++h) {
        if (h < np) {
          u32x4 a0, a1;
#pragma unroll
          for (int p = 0; p < 4; ++p) {
            a0[p] = plo(w[h][2 * p], w[h][2 * p + 1]);
            a1[p] = phi(w[h][2 * p], w[h][2 * p + 1]);
          }
          const int T0 = 2 * (P0 + h), T1 = T0 + 1;
          const unsigned o0 = ((sp * 12 + T0 / 3) * 54 + ks * 3 + T0 % 3) * 1024,
                         o1 = ((sp * 12 + T1 / 3) * 54 + ks * 3 + T1 % 3) * 1024;
          const unsigned vo = (r < 8 ? o0 : o1) + g * 256 + (r & 7) * 16;
          st128(a0, ro, vo, 0);
          st128(a1, ro, vo, 128);
        }
      }
      TS_END(1, ws);
    return;
    }
    const int c1 = wv >> 1, hf = wv & 1, ch = 2 * b + c1;
    char* cb = sm + c1 * CV_CH;
    const rsrc_t rkv = mk(ws + O_KV, ACT), rk = mk(ssk, ACT);
#pragma unroll
    for (int n = 0; n < 2; ++n) {
      const int c = (hf * 2 + n) * 64 + lane;
      glds16(rkv, cb + CV_KV + 512 + (hf * 2 + n) * 1024, (c >> 2) * 128 + (c & 3) * 16, b * 8192 + c1 * 64);
    }
    u32x4 kc[2];
    unsigned kn[2];
#pragma unroll
    for (int n = 0; n < 2; ++n) {
      const int c = (hf * 2 + n) * 64 + lane;
      kc[n] = ld128(rk, ch * 4096 + c * 16, 0);
      kn[n] = ld32(rk, ch * 4096 + c * 16 + 16, 0);
    }
    const unsigned dd = ld32(mk(ssd, D_ * 2), b * 4, 0);
    if (hf == 0) {
      if (lane < 32) *(u32x4*)(cb + CV_KV + lane * 16) = u32x4{0, 0, 0, 0};
      if (lane < 2) *(u32x4*)(cb + CV_E + lane * 16) = u32x4{0, 0, 0, 0};
      if (lane == 0) {
        *(u32x4*)(cb + CV_O) = u32x4{0, 0, 0, 0};
        *(u32x4*)(cb + CV_O + 16) = u32x4{0, 0, 0, kc[0][0] << 16};
      }
    }
#pragma unroll
    for (int n = 0; n < 2; ++n) {
      const int c = (hf * 2 + n) * 64 + lane;
      *(u32x4*)(cb + CV_E + 32 + c * 16) = kc[n];
      const u32x4 o = {__builtin_amdgcn_alignbyte(kc[n][1], kc[n][0], 2), __builtin_amdgcn_alignbyte(kc[n][2], kc[n][1], 2),
                       __builtin_amdgcn_alignbyte(kc[n][3], kc[n][2], 2), __builtin_amdgcn_alignbyte(kn[n], kc[n][3], 2)};
      *(u32x4*)(cb + CV_O + 32 + c * 16) = o;
    }
    TS(2);
    WAITVM(0);
    BAR();
    TS(3);
    f32x4 acc[8] = {};
    const unsigned* ka = (const unsigned*)(cb + ((r & 1) ? CV_E : CV_O)) + ((r + 1) >> 1) + 4 * g + hf * 64;
    const char* kb = cb + CV_KV + 512 + (2 * r + 1 - g - hf * 16) * 16;
    {
      u32x4 A[32], B[32];
#pragma unroll
      for (int j = -CDP; j < 32; ++j) {
        const int n = j + CDP;
        if (n < 32) {
          const unsigned* p = ka + (n >> 3) * 16 + (7 - (n & 7)) * 128;
          A[n] = u32x4{p[0], p[1], p[2], p[3]};
          B[n] = *(const u32x4*)(kb + ((n & 7) * 32 - (n >> 3) * 4) * 16);
        }
        FENCE();
        if (j >= 0) {
#pragma unroll
          for (int m = 0; m <= (j & 7); ++m) acc[7 - (j & 7) + m] = mfma(A[j], B[(j & ~7) + m], acc[7 - (j & 7) + m]);
        }
        FENCE();
      }
    }
#pragma unroll
    for (int n = 0; n < 8; ++n) *(f32x4*)(sm + CV_Y + wv * 8192 + n * 1024 + r * 64 + g * 16) = acc[n];
    TS(4);
    BAR();
    TS(5);
    const rsrc_t rz = mk(ws + O_Z, ACT);
#pragma unroll
    for (int n = 0; n < 2; ++n) {
      const int id = n * 256 + tid, sb = id >> 3, cc = (id >> 2) & 1, c = sb * 4 + (id & 3);
      const char* y0 = sm + CV_Y + cc * 16384 + c * 32;
      const f32x4 ya = *(const f32x4*)y0 + *(const f32x4*)(y0 + 8192);
      const f32x4 yb = *(const f32x4*)(y0 + 16) + *(const f32x4*)(y0 + 8192 + 16);
      const u32x4 kv = *(const u32x4*)(sm + cc * CV_CH + CV_KV + 512 + c * 16);
      const float dv = cc ? bhi(dd) : blo(dd);
      u32x4 o;
#pragma unroll
      for (int p = 0; p < 4; ++p) {
        const unsigned yy = p < 2 ? pk2(ya[2 * p], ya[2 * p + 1]) : pk2(yb[2 * p - 4], yb[2 * p - 3]);
        const unsigned kd = pk2(bhi(kv[3 - p]) * dv, blo(kv[3 - p]) * dv);
        o[p] = pk2(blo(yy) + blo(kd), bhi(yy) + bhi(kd));
      }
      st128(o, rz, sb * (D_ * 64) + (id & 7) * 16, b * 128);
    }
    TS_END(1, ws);
    return;
  }
  b -= K2_CONV;
  {
    const int f = b * 384 + tid;
    const rsrc_t rc = mk(ws + O_CSP, 18 * F_ * 4);
    float s = 0.f;
#pragma unroll
    for (int j = 0; j < 18; ++j) s += __builtin_bit_cast(float, ld32(rc, f * 4, j * F_ * 4));
    ((float*)(ws + O_CS))[f] = s;
    if (f < 3072) *(unsigned*)(ws + O_FLG + (f >> 3) * FSP + (f & 7) * 4) = 0;
    TS_END(1, ws);
  }
}

constexpr int L3_X = 0, L3_H = 73728, L3_CS = 147456, L3_ST = L3_CS + 3072, L3_FL = L3_ST + 1024, L3_S1 = L3_FL + 256,
              L3_Z = L3_S1 + 1024, LDS3 = L3_Z + 9216;
#define RING 6

template <int P0, int P1, int P2> DEVI void prio_by_group(int grp) {
  asm volatile(
      "s_setprio %1\n\t"
      "s_cmp_eq_u32 %0, 0\n\t"
      "s_cbranch_scc1 1f\n\t"
      "s_setprio %2\n\t"
      "s_cmp_eq_u32 %0, 1\n\t"
      "s_cbranch_scc1 1f\n\t"
      "s_setprio %3\n\t"
      "1:" ::"s"(grp), "n"(P0), "n"(P1), "n"(P2)
      : "scc");
}

__global__ __launch_bounds__(768, 3) void k3(const void* __restrict__ q, char* ws, void* __restrict__ out) {
  extern __shared__ __attribute__((aligned(16))) char sm[];
  TS_DECL;
  const int tid = threadIdx.x, lane = tid & 63;
  const int wv = __builtin_amdgcn_readfirstlane(tid >> 6);
  const int r = lane & 15, g = lane >> 4;
#define SPX 2
#define LO 0
#define DO 3
#define NE 1
#define PA 3
#define PB 2
#define PC 0
#define GW 2
#define PS 1
#define PN 0
#define OAUX 0
#define EB 1
#define GP 0
  const int xc = blockIdx.x & 7, ix = blockIdx.x >> 3, grp = wv >> 2;
  const int sp = SPX * (xc % (8 / SPX)) + ix % SPX, ml = ix / SPX, Mt = (xc / (8 / SPX)) * (32 / SPX) + ml;
  const rsrc_t rw = mk(ws + O_W3, 2 * WBG), rx = mk(ws + O_X1, ACT);
  const unsigned l16 = lane * 16;
  const unsigned wb = (sp * 12 + wv) * 55296;
  f32x2 sv[18];
  if (wv == 11) {
    const rsrc_t rs2 = mk(ws + O_ST2, 18 * S_ * 8);
#pragma unroll
    for (int j = 0; j < 18; ++j) sv[j] = __builtin_bit_cast(f32x2, ld64(rs2, lane * 8, (j * S_ + Mt * 64) * 8));
  }
  if (wv == 10) {
    const rsrc_t rc = mk(ws + O_CS, 20480);
#pragma unroll
    for (int n = 0; n < 3; ++n) glds16(rc, sm + L3_CS + n * 1024, l16, sp * 2304 + n * 1024);
  }
#pragma unroll
  for (int n = 0; n < 6; ++n) {
    const int pc = n * 12 + wv;
    glds16(rx, sm + L3_X + pc * 1024, l16, Mt * 73728 + ((pc & 3) * 18 + (pc >> 2)) * 1024);
  }
  const int hh = sp & 1, wc = grp * 4 + (sp >> 1), tc = wc * 3 + (wv & 3);
  u32x4 xq;
  if ((wv & 3) < 3) glds16(mk(ws + O_Z, ACT), sm + L3_Z + (grp * 3 + (wv & 3)) * 1024, l16, (Mt * 2 + hh) * 36864 + tc * 1024);
  if (wv == 7) glds16(mk(ws + O_ST1, S_ * 8), sm + L3_S1, l16, (Mt * 64 + hh * 32) * 8);
  FENCE();
  if (EB) __builtin_amdgcn_s_barrier();
  FENCE();
  u32x4 a[RING][3];
  auto fill = [&]() __attribute__((always_inline)) {
#pragma unroll
    for (int i = 0; i < RING - 1; ++i)
#pragma unroll
      for (int t = 0; t < 3; ++t) a[i][t] = ld128(rw, l16, wb + (i * 3 + t) * 1024);
    FENCE();
  };
  if (grp < NE) {
    fill();
    asm volatile("s_waitcnt vmcnt(%0)" ::"n"(3 * (RING - 1)) : "memory");
  } else {
    WAITVM(0);
  }
  if (wv == 11) {
    float su = 0.f, sq = 0.f;
#pragma unroll
    for (int j = 0; j < 18; ++j) {
      su += sv[j][0];
      sq += sv[j][1];
    }
    const float mean = su * (1.0f / D_);
    const float var = __builtin_fmaxf(sq * (1.0f / D_) - mean * mean, 0.0f);
    const float sd = rbf(__builtin_amdgcn_sqrtf(rbf(rbf(var) + 1e-5f))), rs = __builtin_amdgcn_rcpf(sd);
    *(f32x4*)(sm + L3_ST + l16) = f32x4{-mean, sd, -2.302208198f * rs, -0.1029432397f * rs * rs * rs};
    if (lane < 8) *(unsigned*)(sm + L3_FL + lane * 4) = 0;
  }
  BAR();
  TS(2);
  prio_by_group<3, 2, 1>(grp);
  if (grp >= NE) fill();

  f32x4 acc[3][4];
  u32x4 x[2][4];
  unsigned lb = l16;
#pragma unroll
  for (int t = 0; t < 4; ++t) x[0][t] = *(const u32x4*)(sm + L3_X + t * 1024 + l16);
  {
    float nm[4];
#pragma unroll
    for (int m = 0; m < 4; ++m) nm[m] = *(const float*)(sm + L3_ST + (m * 16 + r) * 16);
#pragma unroll
    for (int t = 0; t < 3; ++t) {
      const f32x4 c = *(const f32x4*)(sm + L3_CS + (wv * 3 + t) * 64 + g * 16);
#pragma unroll
      for (int m = 0; m < 4; ++m) acc[t][m] = f32x4{c[0] * nm[m], c[1] * nm[m], c[2] * nm[m], c[3] * nm[m]};
    }
  }
  sfor<0, 36>([&](auto ic) __attribute__((always_inline)) {
    constexpr int i = decltype(ic)::v;
    constexpr int nx = i + RING - 1;
    if constexpr (i == 33)
      xq = ld128(mk(q, ACT), (wv & 3) < 3 ? ((g >> 1) * 16 + r) * 1152 + (g & 1) * 16 : 0x80000000u, (Mt * 64 + hh * 32) * 1152 + tc * 32);
    if constexpr (i == 18) {
      TS(3);
      __builtin_amdgcn_s_setprio(GP);
      f32x4 st[4];
#pragma unroll
      for (int m = 0; m < 4; ++m) st[m] = *(const f32x4*)(sm + L3_ST + (m * 16 + r) * 16);
#pragma unroll
      for (int t = 0; t < 3; ++t) {
        const int tl = wv * 3 + t;
        char* hp = sm + L3_H + (tl >> 1) * 4096 + (tl & 1) * 8 + l16;
#pragma unroll
        for (int m = 0; m < 4; ++m) {
          float h[4];
#pragma unroll
          for (int p = 0; p < 4; ++p) {
            const float v = acc[t][m][p];
            const float e = __builtin_amdgcn_exp2f(v * __builtin_fmaf(v * v, st[m][3], st[m][2]));
            h[p] = v * __builtin_amdgcn_rcpf(__builtin_fmaf(e, st[m][1], st[m][1]));
          }
          *(u32x2*)(hp + m * 1024) = u32x2{pk2(h[0], h[1]), pk2(h[2], h[3])};
        }
      }
      asm volatile("s_waitcnt lgkmcnt(0)" ::: "memory");
      sm[L3_FL + wv] = 1;
      if (GP) __builtin_amdgcn_s_setprio(0);
      TS(4);
      wait_flag<0>(L3_FL);
#pragma unroll
      for (int t = 0; t < 4; ++t) x[0][t] = *(const u32x4*)(sm + (24 + t) * 1024 + lb);
    }
    if constexpr (i == 23) wait_flag<4>(L3_FL);
    if constexpr (i == 29) {
      wait_flag<8>(L3_FL);
      prio_by_group<PA, PB, PC>(grp);
    }
    if constexpr (i == 11 || i == 23) asm volatile("v_add_u32 %0, 0xc000, %0" : "+v"(lb));
    FENCE();
#pragma unroll
    for (int j = 0; j < 12; ++j) {
      const int m = j / 3, t = j % 3;
      acc[t][m] = mfma(a[i % RING][t], x[i & 1][m], i == 18 ? f32x4{0, 0, 0, 0} : acc[t][m]);
      FENCE();
      if (j >= LO && j < LO + 3 && nx < 36)
        a[nx % RING][j - LO] = ld128(rw, l16, wb + ((nx < 18 ? nx : nx - 18) * 3 + j - LO) * 1024 + (nx < 18 ? 0 : WBG));
      if (j >= DO && j < DO + 4 && i != 17 && i != 35)
        x[(i + 1) & 1][j - DO] = *(const u32x4*)(sm + (((i + 1) % 12) * 4 + j - DO) * 1024 + lb);
      FENCE();
    }
  });
  TS(5);
  const rsrc_t rp = mk(ws + O_PART, 8 * ACT);
#pragma unroll
  for (int t = 0; t < 3; ++t)
#pragma unroll
    for (int h = 0; h < 2; ++h) {
      const f32x4 v0 = acc[t][2 * h], v1 = acc[t][2 * h + 1];
      const u32x4 o = {pk2(v0[0], v0[1]), pk2(v0[2], v0[3]), pk2(v1[0], v1[1]), pk2(v1[2], v1[3])};
      const int tl = wv * 3 + t;
      if (wv == wc && h == hh)
        *(u32x4*)(sm + L3_X + (tl >> 1) * 4096 + (1 - hh) * 2048 + (tl & 1) * 1024 + l16) = o;
      else
        st128a<16>(o, rp, l16, sp * ACT + Mt * 73728 + (tl * 2 + h) * 1024);
    }
  const rsrc_t rf = mk(ws + O_FLG, 384 * FSP);
  asm volatile("s_waitcnt vmcnt(0) lgkmcnt(0)" ::: "memory");
  if (lane == 0) __builtin_amdgcn_raw_buffer_store_b32(1u, rf, 0, (Mt * 12 + wv) * FSP + sp * 4, 16);
  sm[L3_FL + 12 + wv] = 1;
  if ((wv & 3) < 3) {
    const int row = (g >> 1) * 16 + r;
    const u32x4 x1 = *(const u32x4*)(sm + L3_X + (tc >> 1) * 4096 + hh * 2048 + (tc & 1) * 512 + (g >> 1) * 1024 + (lane & 31) * 16);
    const f32x2 s1 = *(const f32x2*)(sm + L3_S1 + row * 8);
    float o[8], sz[8], xv[8];
#pragma unroll
    for (int j = 0; j < 8; ++j) {
      const float z = blo(*(const unsigned short*)(sm + L3_Z + (grp * 3 + (wv & 3)) * 1024 + (g & 1) * 512 + row * 2 + j * 64));
      xv[j] = (j & 1) ? bhi(xq[j >> 1]) : blo(xq[j >> 1]);
      sz[j] = rbf(rbf(rbf(xv[j] - s1[0]) * s1[1]) * z);
      KEEP(sz[j]);
    }
    if ((GW >> grp) & 1) wait_flag<20>(L3_FL);
    unsigned fl = 0;
    asm volatile(
        "1:\n\t"
        "global_load_dword %0, %1, off sc1\n\t"
        "s_sleep %2\n\t"
        "s_waitcnt vmcnt(%3)\n\t"
        "v_cmp_ne_u32 vcc, 1, %0\n\t"
        "s_cbranch_vccnz 1b"
        : "+v"(fl)
        : "v"(ws + O_FLG + (Mt * 12 + wc) * FSP + (lane & 7) * 4), "n"(PS), "n"(PN)
        : "vcc", "memory");
    u32x4 pp[8];
#pragma unroll
    for (int n = 1; n < 8; ++n)
      pp[n] = __builtin_amdgcn_raw_buffer_load_b128(rp, l16, ((sp + n) & 7) * ACT + Mt * 73728 + (tc * 2 + hh) * 1024, 16);
    pp[0] = *(const u32x4*)(sm + L3_X + (tc >> 1) * 4096 + (1 - hh) * 2048 + (tc & 1) * 1024 + l16);
#pragma unroll
    for (int p = 0; p < 4; ++p) {
      float a0 = 0.f, a1 = 0.f;
#pragma unroll
      for (int n = 0; n < 8; ++n) {
        a0 += (p & 1) ? bhi(pp[n][p >> 1]) : blo(pp[n][p >> 1]);
        a1 += (p & 1) ? bhi(pp[n][2 + (p >> 1)]) : blo(pp[n][2 + (p >> 1)]);
      }
      const u32x2 sw = __builtin_amdgcn_permlane32_swap(__builtin_bit_cast(unsigned, a0), __builtin_bit_cast(unsigned, a1), false, false);
#pragma unroll
      for (int t = 0; t < 2; ++t) {
        const unsigned su = sw[t];
        const float bo = rbf(rbf(__builtin_bit_cast(float, su)) + (t ? bhi(x1[p]) : blo(x1[p])));
        const float gt = rbf(bo * __builtin_amdgcn_rcpf(1.0f + __builtin_amdgcn_exp2f(bo * -1.442695041f)));
        o[2 * p + t] = xv[2 * p + t] + rbf(sz[2 * p + t] * gt);
      }
    }
    asm volatile("" : "+v"(fl), "+v"(o[7]));
    st128a<OAUX>(u32x4{pk2(o[0], o[1]), pk2(o[2], o[3]), pk2(o[4], o[5]), pk2(o[6], o[7])}, mk(out, ACT),
                 row * 1152 + (g & 1) * 16, (Mt * 64 + hh * 32) * 1152 + tc * 32);
  }
  TS_END(2, ws);
}

#if defined(__HIP_DEVICE_COMPILE__) && !defined(PKOK)
#pragma clang attribute pop
#endif

extern "C" void kernel_launch(void* const* d_in, const int* in_sizes, int n_in, void* d_out, int out_size, void* d_ws,
                              size_t ws_size, hipStream_t stream) {
  (void)in_sizes; (void)n_in; (void)out_size; (void)ws_size;
  char* ws = (char*)d_ws;
  (void)hipFuncSetAttribute((const void*)k1, hipFuncAttributeMaxDynamicSharedMemorySize, LDS1);
  (void)hipFuncSetAttribute((const void*)k2, hipFuncAttributeMaxDynamicSharedMemorySize, LDS12);
  (void)hipFuncSetAttribute((const void*)k3, hipFuncAttributeMaxDynamicSharedMemorySize, LDS3);
  k1<<<256, 896, LDS1, stream>>>(d_in[0], d_in[1], d_in[2], d_in[3], d_in[7], d_in[8], d_in[9], ws);
  k2<<<K2_GEMM + K2_CONV + K2_CS, 384, LDS12, stream>>>(d_in[0], d_in[4], d_in[5], d_in[10], ws);
  k3<<<256, 768, LDS3, stream>>>(d_in[0], ws, d_out);
}
```
